```python
import torch

def _softmax(x, axis=-1):
    return torch.softmax(x, dim=axis)
def _gelu(x):
    return torch.nn.functional.gelu(x, approximate="tanh")
def _silu(x):
    return torch.nn.functional.silu(x)
def _sigmoid(x):
    return torch.sigmoid(x)
def _layernorm(x, axis=-1, eps=1e-5):
    m = x.mean(dim=axis, keepdim=True)
    v = x.var(dim=axis, keepdim=True, unbiased=False)
    return (x - m) / torch.sqrt(v + eps)
def _scatter_add(tgt, idx, src, axis=0):
    return tgt.clone().index_add_(axis, idx, src)
def _segment_sum(src, seg, n):
    out = torch.zeros((n,) + src.shape[1:], dtype=src.dtype)
    return out.index_add_(0, seg, src)
def _pad(x, pw):
    flat = tuple(v for pair in reversed(pw) for v in pair)
    return torch.nn.functional.pad(x, flat)

kernel_name = 'sequential_preLN__linear_kernel_hedgehog__swiglu_ffn__prompt'

def setup_inputs():

    _w = (lambda a: (lambda t: t.to(torch.bfloat16) if t.is_floating_point() else t)(torch.as_tensor(a)))
    def _A_setup():
        B, S, D, H = 1, 2048, 576, 32
        Dh = D // H
        return {
            "x": _w((lambda *s: torch.randn(*s, dtype=torch.bfloat16) * 0.05)(B, S, D)),
            "wqkv": _w((lambda *s: torch.randn(*s, dtype=torch.bfloat16) * 0.2)(D, 3 * D)),
            "w_phi": _w((lambda *s: torch.randn(*s, dtype=torch.bfloat16) * 0.2)(H, Dh, Dh) * (Dh ** -0.5)),
            "wo": _w((lambda *s: torch.randn(*s, dtype=torch.bfloat16) * 0.05)(D, D)),
        }
    _A_in = _A_setup()

    def _B_setup():
        B, S, D, F = 1, 2048, 576, 4608
        return {
            "x": _w((lambda *s: torch.randn(*s, dtype=torch.bfloat16) * 0.05)(B, S, D)),
            "w_gate": _w((lambda *s: torch.randn(*s, dtype=torch.bfloat16) * 0.05)(D, F)),
            "w_up": _w((lambda *s: torch.randn(*s, dtype=torch.bfloat16) * 0.05)(D, F)),
            "w_down": _w((lambda *s: torch.randn(*s, dtype=torch.bfloat16) * 0.05)(F, D)),
        }
    _B_in = _B_setup()

    return {
        "A_x": _A_in["x"],
        "A_wqkv": _A_in["wqkv"],
        "A_w_phi": _A_in["w_phi"],
        "A_wo": _A_in["wo"],
        "B_x": _B_in["x"],
        "B_w_gate": _B_in["w_gate"],
        "B_w_up": _B_in["w_up"],
        "B_w_down": _B_in["w_down"],
    }

def reference(A_x, A_wqkv, A_w_phi, A_wo, B_x, B_w_gate, B_w_up, B_w_down):
    def _A_ref(x, wqkv, w_phi, wo):
        B, S, D = x.shape
        H = 32; Dh = D // H
        qkv = (lambda x, *d: x.permute(*d))((x @ wqkv).reshape(B, S, 3, H, Dh), 2, 0, 3, 1, 4)
        q, k, v = qkv[0], qkv[1], qkv[2]
        pq = torch.einsum("bhsd,hdf->bhsf", q, w_phi)
        pk = torch.einsum("bhsd,hdf->bhsf", k, w_phi)
        phi_q = torch.concatenate([_softmax(pq, axis=-1), _softmax(-pq, axis=-1)], axis=-1)
        phi_k = torch.concatenate([_softmax(pk, axis=-1), _softmax(-pk, axis=-1)], axis=-1)
        kv = torch.einsum("bhsf,bhsd->bhfd", phi_k, v)
        z  = torch.einsum("bhsf->bhf", phi_k)
        num = torch.einsum("bhqf,bhfd->bhqd", phi_q, kv)
        den = torch.einsum("bhqf,bhf->bhq", phi_q, z)[..., None] + 1e-6
        o = num / den
        return (lambda x, *d: x.permute(*d))(o, 0, 2, 1, 3).reshape(B, S, D) @ wo

    def _B_ref(x, w_gate, w_up, w_down):
        g = _silu(x @ w_gate)
        u = x @ w_up
        return (g * u) @ w_down

    x = A_x
    h = x + _A_ref(_layernorm(x, axis=-1), A_wqkv, A_w_phi, A_wo)
    h2 = h + _B_ref(_layernorm(h, axis=-1), B_w_gate, B_w_up, B_w_down)
    return h2
```

```cpp
#include <hip/hip_runtime.h>
#include <stdint.h>
#include <utility>
typedef unsigned short u16;
typedef unsigned long long u64;
using u32x2=__attribute__((ext_vector_type(2)))unsigned;
using u32x4=__attribute__((ext_vector_type(4)))unsigned;
using f32x4=__attribute__((ext_vector_type(4)))float;
using f32x16=__attribute__((ext_vector_type(16)))float;
#define DI __device__ __forceinline__
constexpr int S_=2048, D_=576, H_=32, DH_=18, F_=4608;
DI float bf2f(u16 v){ return __uint_as_float(((unsigned)v)<<16); }
DI u16 f2bf(float f){ unsigned u=__float_as_uint(f); u += 0x7fff + ((u>>16)&1); return (u16)(u>>16); }
DI unsigned rfl(unsigned x){ return __builtin_amdgcn_readfirstlane(x); }
DI u32x4 make_srd(const void* p, unsigned bytes){ u64 a=(u64)p; u32x4 s; s[0]=rfl((unsigned)a); s[1]=rfl((unsigned)(a>>32)&0xffffu); s[2]=bytes; s[3]=0x20000u; return s; }
#define WAITL(n) __builtin_amdgcn_s_waitcnt(0xC07F|((n)<<8))
#define WAITV(n) __builtin_amdgcn_s_waitcnt(0x0F70|((n)&15)|(((n)>>4)<<14))
#define BAR() asm volatile("s_barrier" ::: "memory")
template<int OFF> DI void trread(u32x2& d, unsigned addr){ asm volatile("ds_read_b64_tr_b16 %0, %1 offset:%2" : "=v"(d) : "v"(addr), "i"(OFF)); }
template<int OFF> DI void ldsread128(u32x4& d, unsigned addr){ asm volatile("ds_read_b128 %0, %1 offset:%2" : "=v"(d) : "v"(addr), "i"(OFF)); }
template<int OFF> DI void ldsread128f(f32x4& d, unsigned addr){ asm volatile("ds_read_b128 %0, %1 offset:%2" : "=v"(d) : "v"(addr), "i"(OFF)); }
DI u32x4 cat(const u32x2& a,const u32x2& b){ return __builtin_shufflevector(a,b,0,1,2,3); }
DI void glds_m0(unsigned m0, unsigned voff, u32x4 srd, unsigned soff){
  asm volatile("s_mov_b32 m0, %0\n\ts_nop 0\n\tbuffer_load_dwordx4 %1, %2, %3 offen lds" :: "s"(m0), "v"(voff), "s"(srd), "s"(soff) : "memory");
}
template<int OFF> DI void glds_off(unsigned voff, u32x4 srd, unsigned soff){
  asm volatile("buffer_load_dwordx4 %0, %1, %2 offen offset:%3 lds" :: "v"(voff), "s"(srd), "s"(soff), "i"(OFF) : "memory");
}
DI void bstore128(u32x4 v, unsigned voff, u32x4 srd, unsigned soff){
  asm volatile("buffer_store_dwordx4 %0, %1, %2, %3 offen\n\ts_nop 1" :: "v"(v), "v"(voff), "s"(srd), "s"(soff) : "memory");
}
DI void vfma(float& d, float a, float b, float c){ asm volatile("v_fma_f32 %0, %1, %2, %3" : "=v"(d) : "v"(a), "v"(b), "v"(c)); }
DI void vmul(float& d, float a, float b){ asm volatile("v_mul_f32 %0, %1, %2" : "=v"(d) : "v"(a), "v"(b)); }
DI void vexp(float& d){ asm volatile("v_exp_f32 %0, %0" : "+v"(d)); }
DI void vrcp(float& d){ asm volatile("v_rcp_f32 %0, %0" : "+v"(d)); }
DI void vcvtpk(unsigned& d, float a, float b){ asm volatile("v_cvt_pk_bf16_f32 %0, %1, %2" : "=v"(d) : "v"(a), "v"(b)); }

#define STAMP_ARG
#define STAMP_PASS(k)
#define STAMP_DECL
#define STAMPIT(i)
#define STAMP_END(n)
constexpr unsigned WS_FQ=0, WS_KVP=WS_FQ+64*32*2304, WS_OF=WS_KVP+32*32*1536, WS_HF=WS_OF+S_*D_*2, WS_SQ=WS_HF+S_*D_*2, WS_MB=WS_SQ+S_*8*4, WS_MEANX=WS_MB+S_*4,
  WS_WOROW=WS_MEANX+S_*4, WS_SMALL=WS_WOROW+4096, WS_FLAGS=WS_SMALL, WS_ACT=WS_FLAGS+256*512, WS_PART=WS_ACT+S_*F_*2;
using s16x4=__attribute__((ext_vector_type(4)))short;
using bf16x8=__attribute__((ext_vector_type(8)))__bf16;
using f32x2=__attribute__((ext_vector_type(2)))float;
using bf16x2=__attribute__((ext_vector_type(2)))__bf16;
#define LDSP(T,a) ((__attribute__((address_space(3))) T*)(a))
DI u32x2 trb(unsigned a){ return __builtin_bit_cast(u32x2, __builtin_amdgcn_ds_read_tr16_b64_v4i16(LDSP(s16x4,a))); }
DI u32x4 trfrag(unsigned a0, unsigned a1){ return cat(trb(a0),trb(a1)); }
DI f32x16 mfma32(u32x4 a,u32x4 b,f32x16 c){ return __builtin_amdgcn_mfma_f32_32x32x16_bf16(__builtin_bit_cast(bf16x8,a),__builtin_bit_cast(bf16x8,b),c,0,0,0); }
DI f32x4 mfma16(u32x4 a,u32x4 b,f32x4 c){ return __builtin_amdgcn_mfma_f32_16x16x32_bf16(__builtin_bit_cast(bf16x8,a),__builtin_bit_cast(bf16x8,b),c,0,0,0); }
DI unsigned pk2(float lo,float hi){ f32x2 v={lo,hi}; return __builtin_bit_cast(unsigned,__builtin_convertvector(v,bf16x2)); }
DI float lo2f(unsigned v){ return __uint_as_float(v<<16); }
DI float hi2f(unsigned v){ return __uint_as_float(v&0xffff0000u); }
DI float xhalf_max(float v){ auto r=__builtin_amdgcn_permlane32_swap(__float_as_uint(v),__float_as_uint(v),false,false); return fmaxf(__uint_as_float(r[0]),__uint_as_float(r[1])); }
DI float xhalf_min(float v){ auto r=__builtin_amdgcn_permlane32_swap(__float_as_uint(v),__float_as_uint(v),false,false); return fminf(__uint_as_float(r[0]),__uint_as_float(r[1])); }
DI float xhalf_sum(float v){ auto r=__builtin_amdgcn_permlane32_swap(__float_as_uint(v),__float_as_uint(v),false,false); return __uint_as_float(r[0])+__uint_as_float(r[1]); }
DI u32x4 packfrag(const f32x16& a,int s){ return u32x4{pk2(a[8*s],a[8*s+1]),pk2(a[8*s+2],a[8*s+3]),pk2(a[8*s+4],a[8*s+5]),pk2(a[8*s+6],a[8*s+7])}; }
DI void hedgehog(const f32x16& p, unsigned (&o)[9]){
  float mx=p[0], mn=p[0];
  #pragma unroll
  for(int i=1;i<9;i++){ mx=fmaxf(mx,p[i]); mn=fminf(mn,p[i]); }
  mx=xhalf_max(mx); mn=xhalf_min(mn);
  const float L=1.4426950408889634f; const float a=-mx*L, b=mn*L;
  float e[18], s1=0.f, s2=0.f;
  #pragma unroll
  for(int i=0;i<9;i++){ e[i]=__builtin_amdgcn_exp2f(fmaf(p[i],L,a)); e[9+i]=__builtin_amdgcn_exp2f(fmaf(p[i],-L,b)); s1+=e[i]; s2+=e[9+i]; }
  s1=__builtin_amdgcn_rcpf(xhalf_sum(s1)); s2=__builtin_amdgcn_rcpf(xhalf_sum(s2));
  #pragma unroll
  for(int i=0;i<9;i++){ e[i]*=s1; e[9+i]*=s2; }
  #pragma unroll
  for(int i=0;i<9;i++) o[i]=pk2(e[2*i],e[2*i+1]);
}
template<int CTRL> DI float dpp_add(float v){ return v+__builtin_bit_cast(float,__builtin_amdgcn_update_dpp(0,__builtin_bit_cast(int,v),CTRL,0xf,0xf,true)); }
DI float dot2(unsigned a,unsigned b,float c){ return __builtin_amdgcn_fdot2_f32_bf16(__builtin_bit_cast(bf16x2,a),__builtin_bit_cast(bf16x2,b),c,false); }
constexpr int K1_RING=73728, K1_SLOT=13824, K1_WPHI=K1_RING+6*K1_SLOT, K1_LDS=K1_WPHI+4*19*48+64;
constexpr int K1_PK=K1_RING, K1_V=K1_RING+20480+64;
DI void glds54(unsigned m0, unsigned voff, unsigned voff2, u32x4 srd, unsigned soff){
  asm volatile("s_mov_b32 m0, %0\n\ts_mov_b32 exec_hi, 0x3fffff\n\tbuffer_load_dwordx4 %1, %2, %3 offen lds\n\tbuffer_load_dwordx4 %4, %2, %3 offen offset:864 lds\n\ts_mov_b32 exec_hi, -1"
               :: "s"(m0), "v"(voff), "s"(srd), "s"(soff), "v"(voff2) : "memory");
}
#define SB() __builtin_amdgcn_sched_barrier(0)
__global__ __launch_bounds__(512,2) void k1(const u16* __restrict__ x, const u16* __restrict__ wqkv, const u16* __restrict__ wphi, const u16* __restrict__ wo,
                                            char* __restrict__ fq, char* __restrict__ kvp, float* __restrict__ meanx, unsigned* __restrict__ worow, unsigned* __restrict__ flags STAMP_ARG){
  STAMP_DECL STAMPIT(0);
  extern __shared__ __attribute__((aligned(16))) char lds[];
  const int tid=threadIdx.x, lane=tid&63; const int w=rfl(tid>>6);
  const int xcd=blockIdx.x&7, i=blockIdx.x>>3, tb=(xcd>>1)*8+(i&7), hg=(xcd&1)*4+(i>>3);
  const int tt=w>>2, hh=w&3, head=4*hg+hh, off=2*(hh&1);
  const unsigned ldsb=(unsigned)(size_t)lds;
  const u32x4 srdx=make_srd(x+(size_t)tb*64*D_, 64*D_*2), srdw=make_srd(wqkv, D_*3*D_*2);
  { const unsigned vx=(lane>>3)*1152+(((lane&7)^((4*(w&1)+(lane>>4))&7))*16);
    #pragma unroll
    for(int b=0;b<9;b++) glds_m0(ldsb+b*8192+w*1024, vx, srdx, w*8*1152+b*128); }
  unsigned wp[4];
  { const u32x4 srdp=make_srd(wphi+head*324+tt*162, 324);
    #pragma unroll
    for(int k=0;k<2;k++){ const int idx=lane+64*k, d=idx/12, fr=(idx-d*12)*2, reg=(fr&3)+4*(fr>>3); const unsigned o=(d*18+9*((fr>>2)&1)+reg)*2;
      asm volatile("buffer_load_ushort %0, %2, %4, 0 offen\n\tbuffer_load_ushort %1, %3, %4, 0 offen" : "=&v"(wp[2*k]), "=&v"(wp[2*k+1]) : "v"(reg<9? o:0x80000000u), "v"(reg+1<9? o+2:0x80000000u), "s"(srdp)); } }
  const int r27=lane>=27, c27=lane-27*r27;
  const unsigned vw = r27*3456+(c27/9)*1152+(c27%9)*16, vw2=vw+2*3456-864;
  const unsigned sw0=hg*144+w*4*3456, mw0=ldsb+K1_RING+w*1728;
  #pragma unroll
  for(int s=0;s<5;s++) glds54(mw0+s*K1_SLOT, vw, vw2, srdw, sw0+s*32*3456);
  const int h=lane>>5, blk=(lane>>4)&1, q=(lane&15)>>2, p=lane&3;
  unsigned bx[4], bw[2];
  { const int row=32*tt+(lane&31);
    #pragma unroll
    for(int e=0;e<4;e++) bx[e]=ldsb+row*128+(((2*e+h)^((row>>1)&7))*16);
    const int hoff=36*hh-2*off;
    #pragma unroll
    for(int t=0;t<2;t++){ int gi=8*t+4*blk+p; gi=gi>14?14:gi; bw[t]=ldsb+K1_RING+(8*h+q)*432+(gi/5)*144+hoff+8*(gi%5); } }
  WAITV(14);
  STAMPIT(1);
  {
    const unsigned a=ldsb+w*1024+lane*16; u32x4 v[9]; float s=0.f, q2=0.f;
    #pragma unroll
    for(int b=0;b<9;b++) v[b]=*LDSP(u32x4,a+b*8192);
    #pragma unroll
    for(int b=0;b<9;b++)
      #pragma unroll
      for(int k=0;k<4;k++){ s=dot2(v[b][k],0x3f803f80u,s); q2=dot2(v[b][k],v[b][k],q2); }
    s=dpp_add<0xB1>(s); q2=dpp_add<0xB1>(q2); s=dpp_add<0x4E>(s); q2=dpp_add<0x4E>(q2); s=dpp_add<0x141>(s); q2=dpp_add<0x141>(q2);
    const float m=s*(1.f/D_); const float r=__builtin_amdgcn_rsqf(fmaxf(q2*(1.f/D_)-m*m,0.f)+1e-5f), mr=-m*r;
    if(hg==0 && (lane&7)==0) meanx[tb*64+w*8+(lane>>3)]=m;
    #pragma unroll
    for(int b=0;b<9;b++){ u32x4 o;
      #pragma unroll
      for(int k=0;k<4;k++) o[k]=pk2(fmaf(lo2f(v[b][k]),r,mr),fmaf(hi2f(v[b][k]),r,mr));
      *LDSP(u32x4,a+b*8192)=o; }
  }
  WAITV(10);
  asm volatile("" : "+v"(wp[0]), "+v"(wp[1]), "+v"(wp[2]), "+v"(wp[3]));
  { const unsigned a=ldsb+K1_WPHI+hh*912+tt*432+lane*4;
    *LDSP(unsigned,a)=wp[0]|(wp[1]<<16);
    if(lane<44+12*tt) *LDSP(unsigned,a+256)=wp[2]|(wp[3]<<16); }
  asm volatile("s_waitcnt vmcnt(6) lgkmcnt(0)\n\ts_barrier" ::: "memory");
  STAMPIT(2);
  f32x16 acc0={}, acc1={};
  u32x4 xf[2][2]; u32x2 al[2][4], ah[2][4];
  #define K1_X(B,E,XB,E2) xf[B][E]=*LDSP(u32x4,bx[2*(E2)+(E)]+(XB)*8192);
  #define K1_A(B,I,SL) { al[B][I]=trb(bw[(I)&1]+(SL)*K1_SLOT+((I)>>1)*6912); ah[B][I]=trb(bw[(I)&1]+(SL)*K1_SLOT+((I)>>1)*6912+1728); }
  K1_X(0,0,0,0) K1_A(0,0,0) K1_A(0,1,0) K1_X(0,1,0,0) K1_A(0,2,0) K1_A(0,3,0)
  u32x4 wq0,wq1,wk0,wk1;
  { const unsigned wb=ldsb+K1_WPHI+hh*912+blk*32+p*8;
    auto wrow=[&](int d){ return wb+((unsigned)d<18u? d:18)*48; };
    const int R=4*h+q;
    wk0=trfrag(wrow(R>=4? R-4-off:99),wrow(R+4-off)); wk1=trfrag(wrow(R+12-off),wrow(99));
    wq0=trfrag(wrow(R-off),wrow(R+8-off)); wq1=trfrag(wrow(R<4? R+16-off:99),wrow(99)); }
  #pragma unroll 1
  for(int s6=0;s6<18;s6+=6){
    #pragma unroll
    for(int j=0;j<6;j++){
      const int s=s6+j; const int c=j&1, n=c^1;
      SB(); acc0=mfma32(cat(al[c][0],ah[c][0]),xf[c][0],acc0); SB();
      glds54(mw0+((j+5)%6)*K1_SLOT, vw, vw2, srdw, (s+5<18)? sw0+(s+5)*32*3456 : 0x80000000u);
      K1_X(n,0,(s+1)>>1,n) K1_A(n,0,(j+1)%6)
      SB(); acc1=mfma32(cat(al[c][1],ah[c][1]),xf[c][0],acc1); SB();
      K1_A(n,1,(j+1)%6) K1_X(n,1,(s+1)>>1,n)
      SB(); acc0=mfma32(cat(al[c][2],ah[c][2]),xf[c][1],acc0); SB();
      K1_A(n,2,(j+1)%6)
      SB(); acc1=mfma32(cat(al[c][3],ah[c][3]),xf[c][1],acc1); SB();
      K1_A(n,3,(j+1)%6)
      SB();
      asm volatile("s_waitcnt vmcnt(6) lgkmcnt(10)\n\ts_barrier" ::: "memory");
    }
  }
  STAMPIT(3);
  u32x4 wr0={0,0,0,0}, wr1={0,0,0,0}; const int worw=blockIdx.x*8+w;
  if(worw<576){ wr0=*(const u32x4*)(wo+worw*D_+lane*8); if(lane<8) wr1=*(const u32x4*)(wo+worw*D_+512+lane*8); }
  unsigned oq[9], ok[9];
  { const u32x4 f00=packfrag(acc0,0), f01=packfrag(acc0,1), f10=packfrag(acc1,0);
    f32x16 z={};
    f32x16 pk=mfma32(wk0,f01,z); f32x16 pq=mfma32(wq0,f00,z);
    pk=mfma32(wk1,f10,pk); pq=mfma32(wq1,f01,pq);
    hedgehog(pk,ok);
    asm volatile("s_waitcnt vmcnt(2)\n\ts_barrier" ::: "memory");
    const int tok=32*tt+(lane&31);
    const unsigned pa=ldsb+K1_PK+hh*5120+tok*80+h*40;
    *LDSP(u32x2,pa)=u32x2{ok[0],ok[1]}; *LDSP(u32x2,pa+8)=u32x2{ok[2],ok[3]}; *LDSP(u32x2,pa+16)=u32x2{ok[4],ok[5]}; *LDSP(u32x2,pa+24)=u32x2{ok[6],ok[7]}; *LDSP(u32x2,pa+32)=u32x2{ok[8],0u};
    const unsigned va=ldsb+K1_V+hh*3072+tok*48+h*8;
    *LDSP(u32x2,va)=u32x2{pk2(acc1[4],acc1[5]),pk2(acc1[6],acc1[7])}; *LDSP(u32x2,va+16)=u32x2{pk2(acc1[8],acc1[9]),pk2(acc1[10],acc1[11])};
    *LDSP(u32x2,va+32)= h? u32x2{0x3f803f80u,0x3f803f80u} : u32x2{pk2(acc1[12],acc1[13]),pk2(acc1[14],acc1[15])};
    asm volatile("s_waitcnt lgkmcnt(0)\n\ts_barrier" ::: "memory");
    f32x16 kv={};
    { const unsigned pa=ldsb+K1_PK+hh*5120+(8*h+q)*80+tt*64+blk*32+p*8, va=ldsb+K1_V+hh*3072+(8*h+q)*48+blk*32+p*8;
      #pragma unroll
      for(int s=0;s<4;s++) kv=mfma32(trfrag(pa+s*1280,pa+s*1280+320),trfrag(va+s*768,va+s*768+192),kv); }
    hedgehog(pq,oq);
    { char* dst=fq+(size_t)(head*64+tb*2+tt)*2304;
      *(u32x4*)(dst+lane*16)=u32x4{oq[0],oq[1],oq[2],oq[3]}; *(u32x4*)(dst+1024+lane*16)=u32x4{oq[4],oq[5],oq[6],oq[7]}; *(unsigned*)(dst+2048+lane*4)=oq[8]; }
    const int vc=lane&31, r19 = vc==20 ? 18 : vc-off;
    char* dst=kvp+(size_t)(head*32+tb)*1536+r19*80+tt*64+8*h;
    if((unsigned)(vc-off)<18u || vc==20){ *(u32x2*)dst=u32x2{pk2(kv[0],kv[1]),pk2(kv[2],kv[3])};
      if(tt==0){ *(u32x2*)(dst+16)=u32x2{pk2(kv[4],kv[5]),pk2(kv[6],kv[7])}; *(u32x2*)(dst+32)=u32x2{pk2(kv[8],kv[9]),pk2(kv[10],kv[11])}; *(u32x2*)(dst+48)=u32x2{pk2(kv[12],kv[13]),pk2(kv[14],kv[15])}; } } }
  if(worw<576){ float s=0.f;
    #pragma unroll
    for(int k=0;k<4;k++){ s=dot2(wr0[k],0x3f803f80u,s); s=dot2(wr1[k],0x3f803f80u,s); }
    s=dpp_add<0xB1>(s); s=dpp_add<0x4E>(s); s=dpp_add<0x141>(s); s=dpp_add<0x140>(s); s+=__shfl_xor(s,16); s=xhalf_sum(s);
    const unsigned hi=pk2(s,0.f)&0xffffu; const unsigned lo=pk2(s-lo2f(hi),0.f);
    const int hd=worw/18, dd=worw-hd*18;
    const int a= dd<16? hd : 32+(hd>>3), hp= dd<16? dd>>3 : (hd>>2)&1, j= dd<16? dd&7 : 2*(hd&3)+(dd-16);
    if(lane==0) worow[32*(a>>1)+16*(j>>2)+4*(2*(a&1)+hp)+(j&3)]=hi|(lo<<16); }
  if(tid<128) flags[blockIdx.x*128+tid]=0;
  STAMPIT(4); STAMP_END(5);
}
constexpr int P1_SLOT=24576, P1_REG=12288;
constexpr int P2_WREG=6656, P2_SLOT=4*P2_WREG, K3_LDS=6*P2_SLOT;
constexpr int FR_SLAB=2*P1_SLOT, FR_KVB=FR_SLAB+576*160;
#define SLAB_EARLY 24
#define GAS __attribute__((address_space(1)))
struct P1 {
  u32x4 x[18][4];
  f32x4 acc[2][2][4];
  float tz[2][4][4], tp[2][4][4];
  u32x2 flo[3][2], fhi[3][2];
  unsigned pk[2][4];
  float c1[4], ir2[4];
  unsigned bA[3], bB[3];
  unsigned voff, stoff;
  u32x4 srdw, srdact;
  unsigned m0base, so_cur, so_next, st_prev, first;
};
struct P2 {
  f32x4 acc[9][4];
  u32x2 flo[6], fhi[6];
  u32x4 bf[2][4], bt[4][4];
  unsigned ba[3][5], bb[3];
  unsigned vw[5], va;
  u32x4 srdw, srda, srdp;
  unsigned m0w, sowb, soab, sow, soa, kk;
  u64 tab;
  GAS unsigned* myflag; GAS unsigned* pflag; unsigned pv;
  unsigned r[4], vl, ownl, pb[2]; u64 om[2];
};
DI void bstore128_sc1(u32x4 v, unsigned voff, u32x4 srd, unsigned soff){
  asm volatile("buffer_store_dwordx4 %0, %1, %2, %3 offen sc1\n\ts_nop 0" :: "v"(v), "v"(voff), "s"(srd), "s"(soff) : "memory");
}
DI void bstore64_sc1(u32x2 v, unsigned voff, u32x4 srd, unsigned soff){
  asm volatile("buffer_store_dwordx2 %0, %1, %2, %3 offen sc1" :: "v"(v), "v"(voff), "s"(srd), "s"(soff) : "memory");
}
DI void bstore32_sc1(unsigned v, unsigned voff, u32x4 srd, unsigned soff){
  asm volatile("buffer_store_dword %0, %1, %2, %3 offen sc1" :: "v"(v), "v"(voff), "s"(srd), "s"(soff) : "memory");
}
template<int OFF> DI void glds_off_sc1(unsigned voff, u32x4 srd, unsigned soff){
  asm volatile("buffer_load_dwordx4 %0, %1, %2 offen offset:%3 sc1 lds" :: "v"(voff), "s"(srd), "s"(soff), "i"(OFF) : "memory");
}
template<int OFF> DI void set_m0(unsigned base){ asm volatile("s_add_u32 m0, %0, %1" :: "s"(base), "i"(OFF) : "scc", "memory"); }
template<int OFF> DI void glds_half(unsigned voff, u32x4 srd, unsigned soff){
  asm volatile("s_mov_b32 exec_hi, 0\n\tbuffer_load_dwordx4 %0, %1, %2 offen offset:%3 lds\n\ts_mov_b32 exec_hi, -1" :: "v"(voff), "s"(srd), "s"(soff), "i"(OFF) : "memory");
}
template<int OFF> DI void ldsread128a(u32x4& d, unsigned addr){ asm volatile("ds_read_b128 %0, %1 offset:%2" : "=a"(d) : "v"(addr), "i"(OFF)); }
template<int OFF,bool SC1> DI void bload128(u32x4& d, unsigned voff, u32x4 srd, unsigned soff){
  if constexpr (SC1) asm volatile("buffer_load_dwordx4 %0, %1, %2, %3 offen offset:%4 sc1" : "=v"(d) : "v"(voff), "s"(srd), "s"(soff), "i"(OFF) : "memory");
  else asm volatile("buffer_load_dwordx4 %0, %1, %2, %3 offen offset:%4" : "=v"(d) : "v"(voff), "s"(srd), "s"(soff), "i"(OFF) : "memory");
}
template<int OFF,bool SC1> DI void bload64(u32x2& d, unsigned voff, u32x4 srd, unsigned soff){
  if constexpr (SC1) asm volatile("buffer_load_dwordx2 %0, %1, %2, %3 offen offset:%4 sc1" : "=v"(d) : "v"(voff), "s"(srd), "s"(soff), "i"(OFF) : "memory");
  else asm volatile("buffer_load_dwordx2 %0, %1, %2, %3 offen offset:%4" : "=v"(d) : "v"(voff), "s"(srd), "s"(soff), "i"(OFF) : "memory");
}
template<int OFF,bool SC1> DI void bload32(unsigned& d, unsigned voff, u32x4 srd, unsigned soff){
  if constexpr (SC1) asm volatile("buffer_load_dword %0, %1, %2, %3 offen offset:%4 sc1" : "=v"(d) : "v"(voff), "s"(srd), "s"(soff), "i"(OFF) : "memory");
  else asm volatile("buffer_load_dword %0, %1, %2, %3 offen offset:%4" : "=v"(d) : "v"(voff), "s"(srd), "s"(soff), "i"(OFF) : "memory");
}
DI void flag_store(GAS unsigned* p, unsigned v){
  asm volatile("s_mov_b64 exec, 1\n\tglobal_store_dword %0, %1, %2 sc1\n\ts_mov_b64 exec, -1" :: "v"(0u), "v"(v), "s"(p) : "memory");
}
DI void flag_load(unsigned& d, GAS unsigned* p){ asm volatile("global_load_dword %0, %1, %2 sc1" : "=v"(d) : "v"(0u), "s"(p) : "memory"); }
DI void flag_check(unsigned& v, GAS unsigned* p, unsigned need){
  unsigned s_, n_;
  asm volatile("s_nop 0\n\tv_readfirstlane_b32 %0, %2\n\ts_cmp_ge_u32 %0, %5\n\ts_cbranch_scc1 .Ldone%=\n\ts_mov_b32 %1, 0x100000\n"
               ".Lspin%=:\n\ts_sleep 2\n\tglobal_load_dword %2, %3, %4 sc1\n\ts_waitcnt vmcnt(0)\n\tv_readfirstlane_b32 %0, %2\n\ts_cmp_ge_u32 %0, %5\n\ts_cbranch_scc1 .Ldone%=\n\t"
               "s_sub_u32 %1, %1, 1\n\ts_cbranch_scc0 .Lspin%=\n.Ldone%=:"
               : "=&s"(s_), "=&s"(n_), "+&v"(v) : "v"(0u), "s"(p), "s"(need) : "scc", "memory");
}
DI void poll_all(GAS unsigned* p, unsigned voff, unsigned skip){
  unsigned v_, n_;
  asm volatile("s_cmp_lg_u32 %4, 0\n\ts_cbranch_scc1 .Ldone%=\n\ts_mov_b32 %1, 0x100000\n"
               ".Lspin%=:\n\tglobal_load_dword %0, %2, %3 sc1\n\ts_waitcnt vmcnt(0)\n\tv_cmp_ne_u32 vcc, 1, %0\n\ts_cbranch_vccz .Ldone%=\n\t"
               "s_sleep 1\n\ts_sub_u32 %1, %1, 1\n\ts_cbranch_scc0 .Lspin%=\n.Ldone%=:"
               : "=&v"(v_), "=&s"(n_) : "v"(voff), "s"(p), "s"(skip) : "scc", "vcc", "memory");
}
template<bool Z,bool AG> DI void p1_mfma(f32x4& acc, const u32x4& a, const u32x4& x){
  if constexpr (Z && AG)  asm volatile("v_mfma_f32_16x16x32_bf16 %0, %1, %2, 0" : "+v"(acc) : "v"(a), "a"(x));
  if constexpr (!Z && AG) asm volatile("v_mfma_f32_16x16x32_bf16 %0, %1, %2, %0" : "+v"(acc) : "v"(a), "a"(x));
  if constexpr (!AG)      asm volatile("v_mfma_f32_16x16x32_bf16 %0, %1, %2, %0" : "+v"(acc) : "v"(a), "v"(x));
}
template<int TL,int I> DI void p1_dr(P1& c){
  constexpr int tt=(I>>2)&3, r=I&3;
  if constexpr (I<16) vmul(c.tz[TL][tt][r], c.acc[TL][0][tt][r], c.c1[tt]); else vmul(c.tp[TL][tt][r], c.acc[TL][0][tt][r], c.acc[TL][1][tt][r]);
}
constexpr int dr_cum(int G){ int n=0; for(int g=0;g<G;g++){ const int m=g&7; n+= m==6?0 : (m&1)?1:2; if(g==1||g==9) n++; } return n; }
template<int TL,int LO,int HI> DI void p1_drs(P1& c){ if constexpr (LO<HI){ p1_dr<TL,LO>(c); p1_drs<TL,LO+1,HI>(c);} }
template<int TT,int HF,int G> DI void p1_grp(P1& c){
  float* z=c.tz[HF][TT]; float* p=c.tp[HF][TT];
  if constexpr (G==0) vexp(z[0]); if constexpr (G==2) vexp(z[1]); if constexpr (G==4) vexp(z[2]); if constexpr (G==8) vexp(z[3]);
  if constexpr (G==5) vfma(z[0],z[0],c.ir2[TT],c.ir2[TT]); if constexpr (G==7) vfma(z[1],z[1],c.ir2[TT],c.ir2[TT]);
  if constexpr (G==9) vfma(z[2],z[2],c.ir2[TT],c.ir2[TT]); if constexpr (G==13) vfma(z[3],z[3],c.ir2[TT],c.ir2[TT]);
  if constexpr (G==10) vrcp(z[0]); if constexpr (G==12) vrcp(z[1]); if constexpr (G==16) vrcp(z[2]); if constexpr (G==18) vrcp(z[3]);
  if constexpr (G==15) vmul(p[0],p[0],z[0]); if constexpr (G==17) vmul(p[1],p[1],z[1]); if constexpr (G==21) vmul(p[2],p[2],z[2]); if constexpr (G==23) vmul(p[3],p[3],z[3]);
}
template<int TT,int HF,int G> DI void p1_cvt(P1& c){ float* p=c.tp[HF][TT];
  if constexpr (G==1) vcvtpk(c.pk[TT&1][2*HF],p[0],p[1]); if constexpr (G==3) vcvtpk(c.pk[TT&1][2*HF+1],p[2],p[3]); }
template<int TT> DI void p1_store(P1& c){ const unsigned* k=c.pk[TT&1]; bstore128_sc1(u32x4{k[0],k[1],k[2],k[3]}, c.stoff, c.srdact, c.st_prev+TT*1024); }
template<int SL> DI void p2_issue(P2& d, int j){
  if(j==0) { set_m0<SL*P2_SLOT>(d.m0w); asm volatile("s_nop 0"); glds_off<0>(d.vw[0],d.srdw,d.sow); }
  if(j==1) glds_off<1024>(d.vw[1],d.srdw,d.sow);
  if(j==2) glds_off<2048>(d.vw[2],d.srdw,d.sow);
  if(j==3) { glds_off<3072>(d.vw[3],d.srdw,d.sow); set_m0<SL*P2_SLOT+4096>(d.m0w); }
  if(j==4) glds_half<0>(d.vw[4],d.srdw,d.sow);
  if(j==5) glds_off_sc1<512>(d.va,d.srda,d.soa);
  if(j==6) glds_off_sc1<1536>(d.va,d.srda,d.soa);
}
DI void p2_next(P2& d){ asm volatile("s_and_b32 %0, %3, 31\n\ts_lshr_b64 %2, %2, 5\n\ts_mul_i32 %1, %0, 0x9000\n\ts_lshl_b32 %0, %0, 13\n\ts_add_u32 %1, %1, %4\n\ts_add_u32 %0, %0, %5"
   : "=&s"(d.soa), "=&s"(d.sow), "+s"(d.tab) : "s"((unsigned)d.tab), "s"(d.sowb), "s"(d.soab) : "scc"); }
template<int ST,int PC> DI void p1_glds(P1& c){ constexpr int SL=(ST+5)%6; const unsigned so=(ST==0? c.so_cur : c.so_next) + (unsigned)((96*SL+8*PC)*9216-(PC&3)*1024); glds_off<(PC&3)*1024>(c.voff, c.srdw, so); }
template<int ST,int TL,int KK,int M,bool FULL> DI void p1_gap(P1& c, P2& d){
  constexpr int G=8*KK+M, SG=2*ST+TL, K=3*ST+KK;
  { const u32x4 a=cat(c.flo[KK][M>>2],c.fhi[KK][M>>2]); p1_mfma<K==0,(K<16)>(c.acc[TL][M>>2][M&3], a, c.x[K][M&3]); }
  if constexpr (M&1) {
    constexpr int N=(KK+2)%3; constexpr bool same=KK==0, toB= !same && TL==0 && FULL; constexpr int S2=(same||toB)? ST : (ST+1)%6;
    constexpr int IMM=(S2&1)*P1_SLOT+(M>>2)*P1_REG+N*4096+((M>>1)&1)*2048;
    const unsigned b=((same&&TL)||toB)? c.bB[S2>>1] : c.bA[S2>>1];
    if constexpr (FULL || ST<5 || same) { if constexpr ((M>>1)&1) trread<IMM>(c.fhi[N][M>>2], b); else trread<IMM>(c.flo[N][M>>2], b); }
  }
  if constexpr (FULL) {
    constexpr int PC=3*TL+KK;
    if constexpr (M==4 && (PC==0||PC==4)) set_m0<((ST+5)%6)*P1_SLOT+PC*1024>(c.m0base);
    if constexpr (M==6) p1_glds<ST,PC>(c);
    if constexpr (SG==11||SG==0) p1_drs<(SG==0),dr_cum(G),dr_cum(G+1)>(c);
    if constexpr (SG>=1 && SG<=8) p1_grp<((SG-1)>>1),((SG-1)&1),G>(c);
    if constexpr (SG>=2 && SG<=9) p1_cvt<((SG-2)>>1),((SG-2)&1),G>(c);
    if constexpr (G==20 && (SG==3||SG==5||SG==7||SG==9)) p1_store<(SG-3)/2>(c);
  } else {
    if constexpr (ST==0) { p1_drs<1,dr_cum(G),dr_cum(G+1)>(c);
      if constexpr (M==1 && KK!=1) set_m0<5*P1_SLOT+KK*2048>(c.m0base);
      if constexpr (M==2) p1_glds<0,2*KK>(c); if constexpr (M==6) p1_glds<0,2*KK+1>(c); }
    if constexpr (ST>=1 && ST<=4) { p1_grp<ST-1,0,G>(c); p1_grp<ST-1,1,(G^1)>(c); }
    if constexpr (ST>=2) { p1_cvt<ST-2,0,G>(c); p1_cvt<ST-2,1,(G^1)>(c); }
    if constexpr (ST>=2) { constexpr int j= G==6?0: G==7?1: G==14?2: G==15?3: G==20?4: G==22?5: G==23?6: -1; if constexpr (j>=0) p2_issue<ST-2>(d,j);
      if constexpr (G==21) p1_store<ST-2>(c); if constexpr (G==23) p2_next(d); }
  }
}
template<int ST,int TL,int KK,bool FULL> DI void p1_step(P1& c, P2& d){
  constexpr bool LAST=!FULL && ST==5;
  WAITL(LAST&&KK==2? 2:6);
  p1_gap<ST,TL,KK,0,FULL>(c,d); p1_gap<ST,TL,KK,1,FULL>(c,d); p1_gap<ST,TL,KK,2,FULL>(c,d); p1_gap<ST,TL,KK,3,FULL>(c,d);
  WAITL(LAST&&KK==2? 0: LAST&&KK==1? 4:6);
  p1_gap<ST,TL,KK,4,FULL>(c,d); p1_gap<ST,TL,KK,5,FULL>(c,d); p1_gap<ST,TL,KK,6,FULL>(c,d); p1_gap<ST,TL,KK,7,FULL>(c,d);
}
template<int ST,int TL,bool FULL> DI void p1_section(P1& c, P2& d){ p1_step<ST,TL,0,FULL>(c,d); p1_step<ST,TL,1,FULL>(c,d); p1_step<ST,TL,2,FULL>(c,d); }
template<int ST,bool FULL> DI void p1_stage(P1& c, P2& d, unsigned bA_next){
  if constexpr (!FULL && ST==2) flag_load(d.pv,d.pflag);
  p1_section<ST,0,FULL>(c,d);
  if constexpr (ST==5 && FULL) {
    #pragma unroll
    for(int k=0;k<3;k++) c.bA[k]=bA_next+k*2*P1_SLOT; }
  if constexpr (FULL) { p1_section<ST,1,true>(c,d); constexpr int N = ST<2?19: ST==2?20: ST<5?21:20;
    if constexpr (ST<4) { constexpr int N0=54-5*ST;
      asm volatile("s_cmp_eq_u32 %0, 0\n\ts_cbranch_scc1 .Lf%=\n\ts_waitcnt vmcnt(%1)\n.Lf%=:\n\ts_waitcnt vmcnt(%2)" :: "s"(c.first), "i"(N), "i"(N0) : "scc", "memory"); }
    else WAITV(N); }
  else { if constexpr (ST==0) WAITV(19); if constexpr (ST==1) WAITV(12); if constexpr (ST==2) { WAITV(8); flag_check(d.pv,d.pflag,1); } if constexpr (ST==5) WAITV(16); }
  BAR();
}
template<bool FULL> DI void p1_unit(P1& c, P2& d, unsigned bn){ p1_stage<0,FULL>(c,d,bn); p1_stage<1,FULL>(c,d,bn); p1_stage<2,FULL>(c,d,bn); p1_stage<3,FULL>(c,d,bn); p1_stage<4,FULL>(c,d,bn); p1_stage<5,FULL>(c,d,bn); }
template<int I> DI void p1_fin(P1& c){
  if constexpr (I<108) { constexpr int tt=I/27, k=I%27, r=k&3; f32x4 &g=c.acc[0][0][tt], &u=c.acc[0][1][tt]; float t;
    if constexpr (k<4) { vmul(t,g[r],u[r]); u[r]=t; }
    else if constexpr (k<8) { vmul(t,g[r],c.c1[tt]); g[r]=t; }
    else if constexpr (k<12) { t=g[r]; vexp(t); g[r]=t; }
    else if constexpr (k<16) { vfma(t,g[r],c.ir2[tt],c.ir2[tt]); g[r]=t; }
    else if constexpr (k<20) { t=g[r]; vrcp(t); g[r]=t; }
    else if constexpr (k<24) { vmul(t,u[r],g[r]); u[r]=t; }
    else if constexpr (k<26) vcvtpk(c.pk[tt&1][k-24],u[2*(k-24)],u[2*(k-24)+1]);
    else bstore64_sc1(u32x2{c.pk[tt&1][0],c.pk[tt&1][1]}, c.stoff, c.srdact, c.st_prev+tt*1024); }
}
template<bool Z> DI void p2_mfma(f32x4& acc, const u32x4& a, const u32x4& b){
  if constexpr (Z) asm volatile("v_mfma_f32_16x16x32_bf16 %0, %1, %2, 0" : "=&v"(acc) : "v"(a), "a"(b));
  else asm volatile("v_mfma_f32_16x16x32_bf16 %0, %1, %2, %0" : "+v"(acc) : "v"(a), "a"(b)); }
template<bool E,int I> DI void p2_fin(P1& e){ if constexpr (E) { p1_fin<3*I>(e); p1_fin<3*I+1>(e); p1_fin<3*I+2>(e); } }
template<int P,int J,bool Z> DI void p2_step(P2& c, P1& e){
  constexpr int PAR=P&1, ST=PAR*9+J, CUR=ST%6, NX=(ST+3)%6; constexpr int JN=(J+3)%9; constexpr int PN=(J+3<9)?P:(P+1)%6;
  constexpr int N = J<3?4: J==3?5: J==4?6: J<7?7: J==7?6:5;
  constexpr int IMM=(PN&1)*P2_SLOT+(JN<8?(JN>>2)*1024:0); constexpr int BI=JN<8?(JN&3):4;
  constexpr int P1n=(P+1)%6, PF=(P+5)%6;
  WAITL(N);
  u32x4 a=cat(c.flo[CUR],c.fhi[CUR]);
  p2_mfma<Z>(c.acc[J][0],a,c.bf[PAR][0]);
  trread<IMM>(c.flo[NX], c.ba[PN>>1][BI]);
  p2_fin<Z,4*J>(e);
  p2_mfma<Z>(c.acc[J][1],a,c.bf[PAR][1]);
  trread<IMM+2*P2_WREG>(c.fhi[NX], c.ba[PN>>1][BI]);
  p2_fin<Z,4*J+1>(e);
  p2_mfma<Z>(c.acc[J][2],a,c.bf[PAR][2]);
  if constexpr (J>=2 && J<=5) ldsread128a<(P1n&1)*P2_SLOT+((J-2)>>1)*P2_WREG+((J-2)&1)*1024>(c.bf[PAR^1][J-2], c.bb[P1n>>1]);
  if constexpr (J==0) set_m0<PF*P2_SLOT>(c.m0w);
  if constexpr (J==7) asm volatile("s_bfe_u32 %0, %1, %2" : "=s"(c.kk) : "s"((unsigned)c.tab), "i"(5<<16) : "scc");
  if constexpr (J==8) asm volatile("s_mul_i32 %0, %1, 0x9000" : "=s"(c.sow) : "s"(c.kk));
  p2_fin<Z,4*J+2>(e);
  p2_mfma<Z>(c.acc[J][3],a,c.bf[PAR][3]);
  if constexpr (J<=3) glds_off<J*1024>(c.vw[J], c.srdw, c.sow);
  if constexpr (J==3) set_m0<PF*P2_SLOT+4096>(c.m0w);
  if constexpr (J==4) glds_half<0>(c.vw[4], c.srdw, c.sow);
  if constexpr (J==5) glds_off_sc1<512>(c.va, c.srda, c.soa);
  if constexpr (J==6) glds_off_sc1<1536>(c.va, c.srda, c.soa);
  if constexpr (J==7) { asm volatile("s_lshl_b32 %0, %1, 13\n\ts_lshr_b64 %2, %2, 5" : "=&s"(c.soa), "+s"(c.kk), "+s"(c.tab) :: "scc"); }
  if constexpr (J==8) { asm volatile("s_add_u32 %0, %0, %2\n\ts_add_u32 %1, %1, %3" : "+s"(c.sow), "+s"(c.soa) : "s"(c.sowb), "s"(c.soab) : "scc"); }
  p2_fin<Z,4*J+3>(e);
}
template<int P,bool Z> DI void p2_stage(P2& c, P1& e){
  if constexpr (P==3) flag_store(c.myflag,2);
  if constexpr (P==5) flag_load(c.pv,c.pflag);
  p2_step<P,0,Z>(c,e); p2_step<P,1,Z>(c,e); p2_step<P,2,Z>(c,e); p2_step<P,3,Z>(c,e); p2_step<P,4,Z>(c,e); p2_step<P,5,Z>(c,e); p2_step<P,6,Z>(c,e); p2_step<P,7,Z>(c,e); p2_step<P,8,Z>(c,e);
  if constexpr (Z) WAITV(26); else if constexpr (P==2) WAITV(14); else if constexpr (P==5) { WAITV(7); flag_check(c.pv,c.pflag,2); } else WAITV(21);
  BAR();
}
template<int J,int U,int K> DI void p2_dump(P2& c){
  if constexpr (K<4) vcvtpk(c.r[K],c.acc[J][2*U+(K>>1)][2*(K&1)],c.acc[J][2*U+(K>>1)][2*(K&1)+1]);
  else asm volatile("buffer_store_dwordx4 %0, %1, %2, %3 offen sc1\n\ts_mov_b64 exec, %4\n\tds_write_b128 %5, %0 offset:%6\n\ts_mov_b64 exec, -1"
                    :: "v"(u32x4{c.r[0],c.r[1],c.r[2],c.r[3]}), "v"(c.vl), "s"(c.srdp), "s"(c.pb[U]+J*1024), "s"(c.om[U]), "v"(c.ownl), "i"(J*1024) : "memory");
}
template<int GI,int I> DI void p2_dumpq(P2& c){ if constexpr (GI>=0 && I>=0 && I<30) p2_dump<3*GI+I/10,(I/5)&1,I%5>(c); }
template<int GI,int S,int JJ> DI void p2_tstep(P2& c){
  constexpr int n=(GI*5+S)*3+JJ, CUR=(n+3)%6, NX=n%6, J=3*GI+JJ;
  constexpr int SN=S<4? S+1:0, JN=S<4? J : J+3, PN=SN+1;
  constexpr int IMM=(PN&1)*P2_SLOT+(JN<8?(JN>>2)*1024:0); constexpr int BI=JN<8?(JN&3):4;
  constexpr bool RD=n+3<45;
  if constexpr (GI==0) { if constexpr (JJ==0) WAITL(S==0?4:2); if constexpr (JJ==1) WAITL(S==0?6:S<4?8:4); if constexpr (JJ==2) WAITL(S<4?8:4); }
  else WAITL(n<43? 4 : n==43? 2:0);
  const u32x4 a=cat(c.flo[CUR],c.fhi[CUR]);
  const u32x4* b= S==0? c.bf[1] : c.bt[S-1<0?0:S-1];
  p2_mfma<false>(c.acc[J][0],a,b[0]);
  if constexpr (RD) trread<IMM>(c.flo[NX], c.ba[PN>>1][BI]);
  p2_mfma<false>(c.acc[J][1],a,b[1]);
  if constexpr (RD) trread<IMM+2*P2_WREG>(c.fhi[NX], c.ba[PN>>1][BI]);
  p2_mfma<false>(c.acc[J][2],a,b[2]);
  if constexpr (GI==0 && S<4 && JJ<2) ldsread128a<((S+2)&1)*P2_SLOT+JJ*P2_WREG>(c.bt[S][2*JJ], c.bb[(S+2)>>1]);
  p2_dumpq<GI-1,(S*3+JJ-1)*3>(c); p2_dumpq<GI-1,(S*3+JJ-1)*3+1>(c);
  p2_mfma<false>(c.acc[J][3],a,b[3]);
  if constexpr (GI==0 && S<4 && JJ<2) ldsread128a<((S+2)&1)*P2_SLOT+JJ*P2_WREG+1024>(c.bt[S][2*JJ+1], c.bb[(S+2)>>1]);
  p2_dumpq<GI-1,(S*3+JJ-1)*3+2>(c);
}
template<int GI,int S> DI void p2_tstage(P2& c){ p2_tstep<GI,S,0>(c); p2_tstep<GI,S,1>(c); p2_tstep<GI,S,2>(c);
  if constexpr (GI==0 && S<3) { WAITV(14-7*S); BAR(); } }
template<int GI> DI void p2_tgroup(P2& c){ p2_tstage<GI,0>(c); p2_tstage<GI,1>(c); p2_tstage<GI,2>(c); p2_tstage<GI,3>(c); p2_tstage<GI,4>(c); }
template<int I> DI void p2_dump_all(P2& c){ if constexpr (I<30){ p2_dumpq<2,I>(c); p2_dump_all<I+1>(c); } }
DI float row_bcast2(float v){
  auto a=__builtin_amdgcn_permlane32_swap(__float_as_uint(v),__float_as_uint(v),false,false);
  auto b=__builtin_amdgcn_permlane16_swap(a[1],a[1],false,false); return __uint_as_float(b[0]); }
DI float rows_sum(float v){ auto b=__builtin_amdgcn_permlane16_swap(__float_as_uint(v),__float_as_uint(v),false,false); return xhalf_sum(__uint_as_float(b[0])+__uint_as_float(b[1])); }
template<bool LO> DI unsigned slab_voff(int pc, int rr, int cc){
  const int R=6*pc+rr, S=R>>5, g=(R>>2)&3, j=4*((R>>4)&1)+(R&3), a=2*S+(g>>1), hp=g&1;
  const bool lo= LO || a<32; const int row=18*(lo? a : 8*(a-32)+4*hp+(j>>1))+(lo? 8*hp+j : 16+(j&1));
  return row*1152+cc*16; }
DI void slab_glds(unsigned m0, unsigned voff, u32x4 srd, unsigned soff){
  asm volatile("s_mov_b32 m0, %0\n\ts_mov_b64 exec, %4\n\tbuffer_load_dwordx4 %1, %2, %3 offen lds\n\ts_mov_b64 exec, -1" :: "s"(m0), "v"(voff), "s"(srd), "s"(soff), "s"(0x07FDFF7FDFF7FDFFull) : "memory"); }

__global__ __launch_bounds__(256,1) void k3(const u16* __restrict__ x, const u16* __restrict__ wo, const u16* __restrict__ wg, const u16* __restrict__ wu, const u16* __restrict__ wd,
                                            char* __restrict__ ws, u16* __restrict__ out STAMP_ARG){
  STAMP_DECL STAMPIT(0);
  extern __shared__ __attribute__((aligned(16))) char lds[];
  const int tid=threadIdx.x, lane=tid&63; const int w=rfl(tid>>6);
  const int xcd=blockIdx.x&7, i=blockIdx.x>>3, T=i>>1, isB=i&1;
  const unsigned ldsb=(unsigned)(size_t)lds;
  GAS unsigned* flags=(GAS unsigned*)(ws+WS_FLAGS);
  const u32x4 srdws=make_srd(ws, WS_SMALL);
  const int g=lane>>4, l15=lane&15, q=l15>>2, p=lane&3;
  P1 c; P2 d;
  unsigned selL=0x3f80u, selH=0x3f800000u; asm volatile("" : "+s"(selL), "+s"(selH));
  const int th=w>>1, fh=w&1;
  c.srdw=make_srd(th? wu:wg, D_*F_*2);
  { const int cc=lane&7; c.voff=(lane>>3)*9216+(((cc>>1)^((lane>>4)&3))*32)+(cc&1)*16; }
  c.m0base=ldsb+th*P1_REG+fh*6*1024;
  const unsigned rowoff=(unsigned)(fh*48)*9216u;
  auto unit_of=[&](int it){ return it<4 ? (isB?5:0)+((it+T)&3) : 4; };
  auto so_of=[&](int it){ return (unsigned)((xcd*9+unit_of(it))*128)+rowoff; };
  #define WST(s) _Pragma("unroll") for(int j=0;j<6;j++) glds_m0(c.m0base+(s)*P1_SLOT+j*1024, c.voff, c.srdw, so_of(0)+(unsigned)(96*(s)+8*j)*9216u);
  u32x2 xr[5]; unsigned mx;
  {
    const int head=(i&3)*8+xcd, tr=i>>2; const unsigned vl=lane*16;
    u32x4 pv[12], q0[2], q1[2]; unsigned q2[2], wr[3];
    #define LD_(k) bload128<((k)&3)*1024,false>(pv[k], vl, srdws, WS_KVP+head*49152+w*12288+((k)>>2)*4096);
    LD_(0) LD_(1) LD_(2) LD_(3) LD_(4) LD_(5) LD_(6) LD_(7) LD_(8) LD_(9) LD_(10) LD_(11)
    #undef LD_
    #pragma unroll
    for(int t=0;t<2;t++){ const unsigned so=WS_FQ+(head*64+tr*8+w*2+t)*2304; bload128<0,false>(q0[t],vl,srdws,so); bload128<1024,false>(q1[t],vl,srdws,so); bload32<2048,false>(q2[t],lane*4,srdws,so); }
    bload32<0,false>(wr[0],tid*4,srdws,WS_WOROW); bload32<1024,false>(wr[1],tid*4,srdws,WS_WOROW); bload32<2048,false>(wr[2],tid*4,srdws,WS_WOROW);
    const u32x4 srdo=make_srd(wo, D_*D_*2); const int rr=lane/10, cc=lane-rr*10;
    unsigned sv[4];
    #pragma unroll
    for(int k=0;k<4;k++) sv[k]=slab_voff<true>(w+4*k,rr,cc);
    #define SLAB(lo,hi) _Pragma("unroll") for(int it=lo;it<hi;it++) slab_glds(ldsb+FR_SLAB+(w+4*it)*960, it<20? sv[it&3] : slab_voff<false>(w+4*it,rr,cc), srdo, xcd*144+(it<20?(it>>2)*108*1152:0));
    SLAB(0,SLAB_EARLY)
    { const u32x4 srdx=make_srd(x+(size_t)(i*64+w*16)*D_+xcd*72, 16*D_*2); const unsigned vx=l15*1152+g*8;
      bload64<0,false>(xr[0],vx,srdx,0); bload64<32,false>(xr[1],vx,srdx,0); bload64<64,false>(xr[2],vx,srdx,0); bload64<96,false>(xr[3],vx,srdx,0); bload64<128,false>(xr[4],g<2?vx:0x80000000u,srdx,0);
      bload32<0,false>(mx,l15*4,srdws,WS_MEANX+(i*64+w*16)*4); }
    WAITV(15+SLAB_EARLY);
    STAMPIT(1);
    { float s[3][8];
      #pragma unroll
      for(int k=0;k<12;k++){ asm volatile("" : "+v"(pv[k]));
        #pragma unroll
        for(int e=0;e<4;e++){ s[k%3][2*e]=dot2(pv[k][e],selL,k<3?0.f:s[k%3][2*e]); s[k%3][2*e+1]=dot2(pv[k][e],selH,k<3?0.f:s[k%3][2*e+1]); } }
      #pragma unroll
      for(int k=0;k<3;k++){ const unsigned a=ldsb+(w*3+k)*2048+lane*16; *LDSP(f32x4,a)=f32x4{s[k][0],s[k][1],s[k][2],s[k][3]}; *LDSP(f32x4,a+1024)=f32x4{s[k][4],s[k][5],s[k][6],s[k][7]}; } }
    asm volatile("s_waitcnt vmcnt(%0) lgkmcnt(0)\n\ts_barrier" :: "i"(6+SLAB_EARLY) : "memory");
    asm volatile("" : "+v"(wr[0]), "+v"(wr[1]), "+v"(wr[2]), "+v"(q0[0]), "+v"(q0[1]), "+v"(q1[0]), "+v"(q1[1]), "+v"(q2[0]), "+v"(q2[1]));
    if(tid<204){
      const int ck=tid>>1, hf=tid&1; u32x2 v={0,0};
      if(ck<95){ const unsigned a0=ldsb+hf*1024+(ck<64? ck*16 : 2048+(ck-64)*16), a1=ldsb+hf*1024+(ck<32? 2048+(ck+32)*16 : 4096+(ck-32)*16);
        f32x4 a=*LDSP(f32x4,a0)+*LDSP(f32x4,a1);
        #pragma unroll
        for(int k=1;k<4;k++) a+=*LDSP(f32x4,a0+k*6144)+*LDSP(f32x4,a1+k*6144);
        v=u32x2{pk2(a[0],a[1]),pk2(a[2],a[3])}; }
      *LDSP(u32x2,ldsb+FR_KVB+tid*8)=v; }
    { const unsigned a=ldsb+FR_SLAB+tid*160+144; *LDSP(u32x4,a)=u32x4{wr[0],0,0,0}; *LDSP(u32x4,a+256*160)=u32x4{wr[1],0,0,0}; if(tid<64) *LDSP(u32x4,a+512*160)=u32x4{wr[2],0,0,0}; }
    asm volatile("s_waitcnt lgkmcnt(0)\n\ts_barrier" ::: "memory");
    const int r=lane&31, h=lane>>5, hf=(r>>2)&1, reg=(r&3)+4*(r>>3); const int r19 = reg<8 ? 8*hf+reg : reg==10 ? 18 : (reg<10 && !hf) ? 8+reg : 19;
    const unsigned ap=ldsb+FR_KVB+r19*80+h*40;
    const u32x2 b0=*LDSP(u32x2,ap), b1=*LDSP(u32x2,ap+8), b2=*LDSP(u32x2,ap+16), b3=*LDSP(u32x2,ap+24); const u32x4 a0=cat(b0,b1), a1=cat(b2,b3); const unsigned a2=*LDSP(unsigned,ap+32);
    #pragma unroll
    for(int t=0;t<2;t++){ f32x16 z={};
      z=mfma32(a0,q0[t],z); z=mfma32(a1,q1[t],z); z=mfma32(u32x4{a2,0,0,0},u32x4{q2[t],0,0,0},z);
      const float rc=__builtin_amdgcn_rcpf(z[10]+1e-6f);
      const unsigned so=WS_OF+(tr*8+w*2+t)*36*1024;
      bstore128_sc1(u32x4{pk2(z[0]*rc,z[1]*rc),pk2(z[2]*rc,z[3]*rc),pk2(z[4]*rc,z[5]*rc),pk2(z[6]*rc,z[7]*rc)}, vl, srdws, so+head*1024);
      bstore32_sc1(pk2(z[8]*rc,z[9]*rc), h? 0x80000000u : vl, srdws, so+(32+(head>>3))*1024+((head>>2)&1)*512+(head&3)*4); }
    WAITV(0);
    flag_store(flags+(tr*4+w)*128+64+head, 1);
    STAMPIT(2);
    SLAB(SLAB_EARLY,24)
    #undef SLAB
  }
  WST(0) WST(1)
  unsigned warm[3];
  { const u32x4 srd= w==2? make_srd(wd+(size_t)xcd*576*D_, 576*D_*2) : make_srd((w?wu:wg)+xcd*576, D_*F_*2-xcd*1152); const unsigned pitch= w==2? 1152:9216;
    const int rr=lane/9; const unsigned v= (lane<54 && w<3)? rr*pitch+(lane-rr*9)*128 : 0x80000000u;
    #pragma unroll
    for(int k=0;k<3;k++) bload32<0,false>(warm[k], v, srd, (i*18+k*6)*pitch); }
  poll_all(flags+i*128+64, (lane&31)*4, w);
  asm volatile("s_waitcnt vmcnt(15)\n\ts_barrier" ::: "memory");
  STAMPIT(3);
  {
    u32x4 o[18];
    { const unsigned vo=(g>>1)*1024+((g&1)*32+(w&1)*16+l15)*16, so=WS_OF+(i*2+(w>>1))*36*1024;
      #define LD_(k) bload128<((k)&1)*2048,true>(o[k], vo, srdws, so+((k)>>1)*4096);
      LD_(0) LD_(1) LD_(2) LD_(3) LD_(4) LD_(5) LD_(6) LD_(7) LD_(8) LD_(9) LD_(10) LD_(11) LD_(12) LD_(13) LD_(14) LD_(15) LD_(16) LD_(17)
      #undef LD_
    }
    f32x4 acc[5]={};
    const unsigned wb=ldsb+FR_SLAB+(4*g+q)*160+p*8;
    u32x4 a[3][5];
    #define K2_RD(S) _Pragma("unroll") for(int j=0;j<5;j++) a[(S)%3][j]=trfrag(wb+(S)*5120+j*32, wb+(S)*5120+2560+j*32);
    K2_RD(0) K2_RD(1)
    #pragma unroll
    for(int S=0;S<18;S++){
      if(S+2<18) { K2_RD(S+2) }
      SB();
      asm volatile("s_waitcnt vmcnt(%1)" : "+v"(o[S]) : "i"(17-S));
      #pragma unroll
      for(int j=0;j<5;j++) acc[j]=mfma16(a[S%3][j],o[S],acc[j]);
      SB(); }
    #undef K2_RD
    BAR();
    STAMPIT(4);
    asm volatile("" : "+v"(xr[0]), "+v"(xr[1]), "+v"(xr[2]), "+v"(xr[3]), "+v"(xr[4]), "+v"(mx), "+v"(warm[0]), "+v"(warm[1]), "+v"(warm[2]));
    const float m=__uint_as_float(mx)+row_bcast2(acc[4][0]+acc[4][1])*(1.f/D_);
    float s2=0.f;
    #pragma unroll
    for(int j=0;j<5;j++){ const float v0=acc[j][0]+(lo2f(xr[j][0])-m), v1=acc[j][1]+(hi2f(xr[j][0])-m), v2=acc[j][2]+(lo2f(xr[j][1])-m), v3=acc[j][3]+(hi2f(xr[j][1])-m);
      const int col=xcd*72+16*j+4*g; const bool ok= j<4 || g<2;
      bstore64_sc1(u32x2{pk2(v0,v1),pk2(v2,v3)}, ok? (unsigned)((col>>5)*1024+(l15+16*((col>>2)&3))*16+((col>>4)&1)*8) : 0x80000000u, srdws, WS_HF+(i*4+w)*18*1024);
      s2+= ok? v0*v0+v1*v1+v2*v2+v3*v3 : 0.f; }
    s2=rows_sum(s2);
    bstore32_sc1(__float_as_uint(g? m:s2), g==0? (unsigned)(WS_SQ+((i*8+xcd)*64+l15*4+w)*4) : (g==1 && xcd==0)? (unsigned)(WS_MB+(i*64+w*16+l15)*4) : 0x80000000u, srdws, 0);
    WAITV(0);
    flag_store(flags+T*256+96+isB*128+xcd*4+w, 1);
    STAMPIT(5);
  }
  WST(2) WST(3) WST(4) WST(5)
  #undef WST
  c.srdact=make_srd(ws+WS_ACT, S_*F_*2);
  const unsigned f2=2*(g&1)+(q>>1);
  const unsigned bfull=ldsb+(4*g+q)*128+(((2*fh)^f2)*32)+p*8, bhalf=ldsb+(4*g+q)*128+(((2*isB+fh)^f2)*32)+p*8;
  #pragma unroll
  for(int k=0;k<3;k++){ c.bA[k]=bfull+k*2*P1_SLOT; c.bB[k]=(bfull^32)+k*2*P1_SLOT; }
  c.stoff=lane*16+th*4096+fh*8192; c.st_prev=0x80000000u;
  #pragma unroll
  for(int k=0;k<4;k++){ asm volatile("" : "=v"(c.tz[0][k][0]), "=v"(c.tz[0][k][1]), "=v"(c.tz[0][k][2]), "=v"(c.tz[0][k][3]), "=v"(c.tp[0][k][0]), "=v"(c.tp[0][k][1]), "=v"(c.tp[0][k][2]), "=v"(c.tp[0][k][3]));
    asm volatile("" : "=v"(c.acc[0][0][k]), "=v"(c.acc[0][1][k]), "=v"(c.acc[1][0][k]), "=v"(c.acc[1][1][k])); }
  const int fc=w&1, ch=isB;
  d.srdw=make_srd((const char*)wd+(size_t)xcd*576*1152-4096, 576*1152+4096);
  d.srda=make_srd(ws+WS_ACT+(size_t)(T*144+xcd*18)*8192-4096, 18*8192+4096);
  d.m0w=ldsb+w*P2_WREG; d.sowb=(unsigned)(8*w*1152); d.soab=(unsigned)(w*2048);
  auto kst=[&](int s){ const int pr=s>>1; const int who = pr<4 ? (pr&1) : (pr>=6); const int j = pr<4 ? (pr>>1) : 2+(pr&1);
    return s<16 ? 2*(((who^isB)?5:0)+((j+T)&3))+(s&1) : 8+((s&1)^isB); };
  u64 tabA=0, tabB=0;
  #pragma unroll
  for(int s=1;s<6;s++) tabA|=(u64)kst(s)<<(5*(s-1));
  #pragma unroll
  for(int s=6;s<18;s++) tabB|=(u64)kst(s)<<(5*(s-6));
  asm volatile("" : "+s"(tabA), "+s"(tabB));
  d.sow=d.sowb+kst(0)*36864u; d.soa=d.soab+kst(0)*8192u; d.tab=tabA;
  d.myflag=flags+blockIdx.x*128; d.pflag=flags+(blockIdx.x^8)*128;
  poll_all(flags+T*256+96, (lane&31)*4+(lane>>5)*512, w);
  BAR();
  STAMPIT(6);
  { u32x4 sv[2]; const unsigned vl=lane*16;
    bload128<0,true>(sv[0], l15*16+g*512, srdws, WS_SQ+(T*2+th)*2048); bload128<256,true>(sv[1], l15*16+g*512, srdws, WS_SQ+(T*2+th)*2048);
    #pragma unroll
    for(int k=0;k<18;k++)
      #pragma unroll
      for(int t=0;t<4;t++){ const unsigned so=WS_HF+((T*8+th*4+t)*18+(k&~3))*1024;
        if(k<16) asm volatile("buffer_load_dwordx4 %0, %1, %2, %3 offen offset:%4 sc1" : "=a"(c.x[k][t]) : "v"(vl), "s"(srdws), "s"(so), "i"((k&3)*1024));
        else     asm volatile("buffer_load_dwordx4 %0, %1, %2, %3 offen offset:%4 sc1" : "=v"(c.x[k][t]) : "v"(vl), "s"(srdws), "s"(so), "i"((k&3)*1024)); }
    WAITV(60);
    asm volatile("" : "+v"(sv[0]), "+v"(sv[1]));
    #pragma unroll
    for(int t=0;t<4;t++){ const float v=rows_sum(__uint_as_float(sv[0][t])+__uint_as_float(sv[1][t]))*(1.f/D_)+1e-5f;
      c.ir2[t]=v; c.c1[t]=-1.4426950408889634f*__builtin_amdgcn_rsqf(v); asm volatile("" : "+v"(c.c1[t]), "+v"(c.ir2[t])); } }
  BAR();
  STAMPIT(7);
  #pragma unroll
  for(int k=0;k<2;k++){ trread<0>(c.flo[k][0],c.bA[0]+k*4096); trread<2048>(c.fhi[k][0],c.bA[0]+k*4096); trread<P1_REG>(c.flo[k][1],c.bA[0]+k*4096); trread<P1_REG+2048>(c.fhi[k][1],c.bA[0]+k*4096); }
  unsigned utab=unit_of(0)|unit_of(1)<<4|unit_of(2)<<8|unit_of(3)<<12|4<<16; const unsigned sob=xcd*9*128+rowoff, stb=(T*144+xcd*18)*8192; c.so_cur=so_of(0);
  asm volatile("" : "+s"(utab));
  #pragma unroll 1
  for(int it=0;it<4;it++){
    const unsigned un=utab&15; utab>>=4;
    c.so_next=sob+(utab&15)*128; c.first=it;
    p1_unit<true>(c, d, bfull^(it==3? bfull^bhalf : 0));
    c.st_prev=stb+un*16384; c.so_cur=c.so_next;
  }
  STAMPIT(8);
  flag_store(d.myflag,1);
  int ln2=threadIdx.x&63; asm volatile("" : "+v"(ln2));
  { const int cc=ln2&7; const unsigned v0=(ln2>>3)*1152+(((cc>>1)^((ln2>>4)&3))*32)+(cc&1)*16+ch*640+4096;
    #pragma unroll
    for(int j=0;j<4;j++) d.vw[j]=v0-j*896;
    const int c4=ln2&3, r=(ln2>>2)&7; d.vw[4]=r*1152+512+ch*64+(((c4>>1)^((r>>2)&1))*32)+(c4&1)*16+4096;
    d.va=ln2*16+4096-512; }
  p1_unit<false>(c, d, 0);
  c.st_prev=(unsigned)((T*144+xcd*18+8+isB)*8192);
  STAMPIT(9);
  #pragma unroll
  for(int j=0;j<7;j++) p2_issue<4>(d,j);
  p2_next(d); d.tab=tabB;
  { asm volatile("" : "+v"(ln2)); const int lane=ln2, q=(lane&15)>>2, p=lane&3; const int g=lane>>4; const int f=2*(g&1)+(q>>1);
    c.stoff=lane*16+th*4096+fh*8;
    #pragma unroll
    for(int sp=0;sp<3;sp++){
      #pragma unroll
      for(int jj=0;jj<4;jj++) d.ba[sp][jj]=ldsb+sp*2*P2_SLOT+(g>>1)*P2_WREG+fc*2048+(4*(g&1)+q)*128+8*p+((jj^f)*32);
      d.ba[sp][4]=ldsb+sp*2*P2_SLOT+(g>>1)*P2_WREG+4096+(4*(g&1)+q)*64+8*p+((fc^(g&1))*32);
      d.bb[sp]=ldsb+sp*2*P2_SLOT+2*th*P2_WREG+4608+lane*16; } }
  ldsread128a<0>(d.bf[0][0],d.bb[0]); ldsread128a<1024>(d.bf[0][1],d.bb[0]); ldsread128a<P2_WREG>(d.bf[0][2],d.bb[0]); ldsread128a<P2_WREG+1024>(d.bf[0][3],d.bb[0]);
  trread<0>(d.flo[0],d.ba[0][0]); trread<2*P2_WREG>(d.fhi[0],d.ba[0][0]); trread<0>(d.flo[1],d.ba[0][1]); trread<2*P2_WREG>(d.fhi[1],d.ba[0][1]); trread<0>(d.flo[2],d.ba[0][2]); trread<2*P2_WREG>(d.fhi[2],d.ba[0][2]);
  p2_stage<0,true>(d,c);
  #pragma unroll 1
  for(int s=0;s<2;s++){ p2_stage<1,false>(d,c); p2_stage<2,false>(d,c); p2_stage<3,false>(d,c); p2_stage<4,false>(d,c); p2_stage<5,false>(d,c); p2_stage<0,false>(d,c); }
  STAMPIT(10);
  asm volatile("" : "+v"(ln2));
  { const int lane=ln2, l15=lane&15, g=lane>>4;
  d.srdp=make_srd(ws+WS_PART, 8*S_*D_*2); d.vl=lane*16; d.ownl=ldsb+lane*16;
  #pragma unroll
  for(int u=0;u<2;u++){     asm volatile("s_cmp_eq_u32 %2, %3\n\ts_cselect_b32 %0, 0x80000000, %4\n\ts_cselect_b64 %1, -1, 0" : "=&s"(d.pb[u]), "=&s"(d.om[u]) : "s"(2*w+u), "s"(xcd), "s"((unsigned)(((((T*2+ch)*8+2*w+u)*8+xcd)*9)*1024)) : "scc"); }
  p2_tgroup<0>(d); p2_tgroup<1>(d);
  const int ow=xcd>>1, ou=xcd&1; const int t16=T*8+(ow>>1)*4+2*ou;
  u32x4 hr[2]; unsigned h8, mA, mB, m8;
  const int ks=ch*10+(ow&1)*4+w;
  bload128<0,true>(hr[0], d.vl, srdws, WS_HF+(t16*18+ks)*1024); bload128<0,true>(hr[1], d.vl, srdws, WS_HF+((t16+1)*18+ks)*1024);
  bload32<0,true>(h8, ((lane>>1)&1)*18*1024+(16*w+(lane>>2))*16+(lane&1)*4, srdws, WS_HF+(t16*18+8+ch)*1024+(ow&1)*8);
  bload32<0,true>(mA, l15*4, srdws, WS_MB+t16*64); bload32<64,true>(mB, l15*4, srdws, WS_MB+t16*64); bload32<0,true>(m8, (((lane>>1)&1)*16+(lane>>2))*4, srdws, WS_MB+t16*64);
  p2_tgroup<2>(d);
  STAMPIT(11);
  asm volatile("s_nop 7" ::: "memory");
  p2_dump_all<0>(d);
  asm volatile("s_waitcnt vmcnt(0) lgkmcnt(0)" ::: "memory");
  asm volatile("s_mov_b64 exec, 3\n\tglobal_store_dword %0, %1, %2 sc1\n\ts_mov_b64 exec, -1" :: "v"(lane*512), "v"(1u), "s"(flags+((i*8+2*w)*128+32+xcd)) : "memory");
  STAMPIT(12);
  poll_all(flags+blockIdx.x*128+32, (lane&7)*4, w);
  BAR();
  STAMPIT(13);
  u32x4 v[8][2]; unsigned v8[8];
  { const unsigned sb=(unsigned)(((T*2+ch)*8+xcd)*8*9*1024);
    #pragma unroll
    for(int s=1;s<8;s++){ const unsigned so=sb+((xcd+s)&7)*9*1024; bload128<0,true>(v[s][0], d.vl, d.srdp, so+w*2048); bload128<1024,true>(v[s][1], d.vl, d.srdp, so+w*2048); bload32<0,true>(v8[s], lane*4, d.srdp, so+8192+w*256); }
    v[0][0]=*LDSP(u32x4,d.ownl+w*2048); v[0][1]=*LDSP(u32x4,d.ownl+w*2048+1024); v8[0]=*LDSP(unsigned,ldsb+8192+w*256+lane*4); }
  asm volatile("" : "+v"(hr[0]), "+v"(hr[1]), "+v"(h8), "+v"(mA), "+v"(mB), "+v"(m8));
  float s[2][8], s8[2];
  #pragma unroll
  for(int k=0;k<2;k++){ const float a=__uint_as_float(mA), b=__uint_as_float(mB);
    s[k][0]=lo2f(hr[0][2*k])+a; s[k][1]=hi2f(hr[0][2*k])+a; s[k][2]=lo2f(hr[0][2*k+1])+a; s[k][3]=hi2f(hr[0][2*k+1])+a;
    s[k][4]=lo2f(hr[1][2*k])+b; s[k][5]=hi2f(hr[1][2*k])+b; s[k][6]=lo2f(hr[1][2*k+1])+b; s[k][7]=hi2f(hr[1][2*k+1])+b; }
  s8[0]=lo2f(h8)+__uint_as_float(m8); s8[1]=hi2f(h8)+__uint_as_float(m8);
  #pragma unroll
  for(int x_=0;x_<8;x_++){
    if(x_) asm volatile("s_waitcnt vmcnt(%3)" : "+v"(v[x_][0]), "+v"(v[x_][1]), "+v"(v8[x_]) : "i"(3*(7-x_)));
    #pragma unroll
    for(int k=0;k<2;k++)
      #pragma unroll
      for(int e=0;e<4;e++){ s[k][2*e]=dot2(v[x_][k][e],selL,s[k][2*e]); s[k][2*e+1]=dot2(v[x_][k][e],selH,s[k][2*e+1]); }
    s8[0]=dot2(v8[x_],selL,s8[0]); s8[1]=dot2(v8[x_],selH,s8[1]); }
  { u16* o=out+(size_t)(t16*16+l15)*D_+ks*32+4*g;
    *(u32x2*)o=u32x2{pk2(s[0][0],s[0][1]),pk2(s[0][2],s[0][3])}; *(u32x2*)(o+16)=u32x2{pk2(s[1][0],s[1][1]),pk2(s[1][2],s[1][3])};
    *(u32x2*)(o+16*D_)=u32x2{pk2(s[0][4],s[0][5]),pk2(s[0][6],s[0][7])}; *(u32x2*)(o+16*D_+16)=u32x2{pk2(s[1][4],s[1][5]),pk2(s[1][6],s[1][7])};
    *(unsigned*)(out+(size_t)(t16*16+((lane>>1)&1)*16+(lane>>2))*D_+256+32*ch+16*(ow&1)+4*w+2*(lane&1))=pk2(s8[0],s8[1]); }
  }
  STAMPIT(14); STAMP_END(15);
}
extern "C" void kernel_launch(void* const* d_in, const int* in_sizes, int n_in, void* d_out, int out_size, void* d_ws, size_t ws_size, hipStream_t stream){
  const u16* x=(const u16*)d_in[0]; const u16* wqkv=(const u16*)d_in[1]; const u16* wphi=(const u16*)d_in[2]; const u16* wo=(const u16*)d_in[3];
  const u16* wg=(const u16*)d_in[5]; const u16* wu=(const u16*)d_in[6]; const u16* wd=(const u16*)d_in[7];
  char* ws=(char*)d_ws;
  (void)hipFuncSetAttribute((const void*)k1, hipFuncAttributeMaxDynamicSharedMemorySize, K1_LDS);
  (void)hipFuncSetAttribute((const void*)k3, hipFuncAttributeMaxDynamicSharedMemorySize, K3_LDS);
  k1<<<256,512,K1_LDS,stream>>>(x,wqkv,wphi,wo,ws+WS_FQ,ws+WS_KVP,(float*)(ws+WS_MEANX),(unsigned*)(ws+WS_WOROW),(unsigned*)(ws+WS_FLAGS) STAMP_PASS(0));
  k3<<<256,256,K3_LDS,stream>>>(x,wo,wg,wu,wd,ws,(u16*)d_out STAMP_PASS(1));
}
```

```cpp
#include <hip/hip_runtime.h>
#include <stdint.h>
#include <utility>
typedef unsigned short u16;
typedef unsigned long long u64;
using u32x2=__attribute__((ext_vector_type(2)))unsigned;
using u32x4=__attribute__((ext_vector_type(4)))unsigned;
using f32x4=__attribute__((ext_vector_type(4)))float;
using f32x16=__attribute__((ext_vector_type(16)))float;
#define DI __device__ __forceinline__
constexpr int S_=2048, D_=576, H_=32, DH_=18, F_=4608;
DI float bf2f(u16 v){ return __uint_as_float(((unsigned)v)<<16); }
DI u16 f2bf(float f){ unsigned u=__float_as_uint(f); u += 0x7fff + ((u>>16)&1); return (u16)(u>>16); }
template<unsigned M,int SH> DI unsigned divm(unsigned x){ return __umul24(x,M)>>SH; }
DI unsigned mad24(unsigned a,unsigned b,unsigned c){ return __umul24(a,b)+c; }
DI unsigned rfl(unsigned x){ return __builtin_amdgcn_readfirstlane(x); }
DI u32x4 make_srd(const void* p, unsigned bytes){ u64 a=(u64)p; u32x4 s; s[0]=rfl((unsigned)a); s[1]=rfl((unsigned)(a>>32)&0xffffu); s[2]=bytes; s[3]=0x20000u; return s; }
#define WAITL(n) __builtin_amdgcn_s_waitcnt(0xC07F|((n)<<8))
#define WAITV(n) __builtin_amdgcn_s_waitcnt(0x0F70|((n)&15)|(((n)>>4)<<14))
#define BAR() asm volatile("s_barrier" ::: "memory")
template<int OFF> DI void trread(u32x2& d, unsigned addr){ asm volatile("ds_read_b64_tr_b16 %0, %1 offset:%2" : "=v"(d) : "v"(addr), "i"(OFF)); }
template<int OFF> DI void ldsread128(u32x4& d, unsigned addr){ asm volatile("ds_read_b128 %0, %1 offset:%2" : "=v"(d) : "v"(addr), "i"(OFF)); }
template<int OFF> DI void ldsread128f(f32x4& d, unsigned addr){ asm volatile("ds_read_b128 %0, %1 offset:%2" : "=v"(d) : "v"(addr), "i"(OFF)); }
DI u32x4 cat(const u32x2& a,const u32x2& b){ return __builtin_shufflevector(a,b,0,1,2,3); }
DI void glds_m0(unsigned m0, unsigned voff, u32x4 srd, unsigned soff){
  asm volatile("s_mov_b32 m0, %0\n\ts_nop 0\n\tbuffer_load_dwordx4 %1, %2, %3 offen lds" :: "s"(m0), "v"(voff), "s"(srd), "s"(soff) : "memory");
}
template<int OFF> DI void glds_off(unsigned voff, u32x4 srd, unsigned soff){
  asm volatile("buffer_load_dwordx4 %0, %1, %2 offen offset:%3 lds" :: "v"(voff), "s"(srd), "s"(soff), "i"(OFF) : "memory");
}
DI void bstore128(u32x4 v, unsigned voff, u32x4 srd, unsigned soff){
  asm volatile("buffer_store_dwordx4 %0, %1, %2, %3 offen\n\ts_nop 1" :: "v"(v), "v"(voff), "s"(srd), "s"(soff) : "memory");
}
DI void vfma(float& d, float a, float b, float c){ asm volatile("v_fma_f32 %0, %1, %2, %3" : "=v"(d) : "v"(a), "v"(b), "v"(c)); }
DI void vmul(float& d, float a, float b){ asm volatile("v_mul_f32 %0, %1, %2" : "=v"(d) : "v"(a), "v"(b)); }
DI void vexp(float& d){ asm volatile("v_exp_f32 %0, %0" : "+v"(d)); }
DI void vrcp(float& d){ asm volatile("v_rcp_f32 %0, %0" : "+v"(d)); }
DI void vcvtpk(unsigned& d, float a, float b){ asm volatile("v_cvt_pk_bf16_f32 %0, %1, %2" : "=v"(d) : "v"(a), "v"(b)); }

#define STAMP_ARG
#define STAMP_PASS(k)
#define STAMP_DECL
#define STAMPIT(i)
#define STAMP_END(n)
constexpr unsigned WS_FQ=0, WS_KVP=WS_FQ+64*32*2304, WS_OF=WS_KVP+32*32*1536, WS_HF=WS_OF+S_*D_*2, WS_SQ=WS_HF+S_*D_*2, WS_MB=WS_SQ+S_*8*4, WS_MEANX=WS_MB+S_*4,
  WS_WOROW=WS_MEANX+S_*4, WS_SMALL=WS_WOROW+8192, WS_FLAGS=WS_SMALL, WS_ACT=WS_FLAGS+256*512, WS_PART=WS_ACT+S_*F_*2;
using s16x4=__attribute__((ext_vector_type(4)))short;
using bf16x8=__attribute__((ext_vector_type(8)))__bf16;
using f32x2=__attribute__((ext_vector_type(2)))float;
using bf16x2=__attribute__((ext_vector_type(2)))__bf16;
#define LDSP(T,a) ((__attribute__((address_space(3))) T*)(a))
DI u32x2 trb(unsigned a){ return __builtin_bit_cast(u32x2, __builtin_amdgcn_ds_read_tr16_b64_v4i16(LDSP(s16x4,a))); }
DI u32x4 trfrag(unsigned a0, unsigned a1){ return cat(trb(a0),trb(a1)); }
DI f32x16 mfma32(u32x4 a,u32x4 b,f32x16 c){ return __builtin_amdgcn_mfma_f32_32x32x16_bf16(__builtin_bit_cast(bf16x8,a),__builtin_bit_cast(bf16x8,b),c,0,0,0); }
DI f32x4 mfma16(u32x4 a,u32x4 b,f32x4 c){ return __builtin_amdgcn_mfma_f32_16x16x32_bf16(__builtin_bit_cast(bf16x8,a),__builtin_bit_cast(bf16x8,b),c,0,0,0); }
DI unsigned pk2(float lo,float hi){ f32x2 v={lo,hi}; return __builtin_bit_cast(unsigned,__builtin_convertvector(v,bf16x2)); }
DI float lo2f(unsigned v){ return __uint_as_float(v<<16); }
DI float hi2f(unsigned v){ return __uint_as_float(v&0xffff0000u); }
DI float xhalf_max(float v){ auto r=__builtin_amdgcn_permlane32_swap(__float_as_uint(v),__float_as_uint(v),false,false); return fmaxf(__uint_as_float(r[0]),__uint_as_float(r[1])); }
DI float xhalf_min(float v){ auto r=__builtin_amdgcn_permlane32_swap(__float_as_uint(v),__float_as_uint(v),false,false); return fminf(__uint_as_float(r[0]),__uint_as_float(r[1])); }
DI float xhalf_sum(float v){ auto r=__builtin_amdgcn_permlane32_swap(__float_as_uint(v),__float_as_uint(v),false,false); return __uint_as_float(r[0])+__uint_as_float(r[1]); }
DI u32x4 packfrag(const f32x16& a,int s){ return u32x4{pk2(a[8*s],a[8*s+1]),pk2(a[8*s+2],a[8*s+3]),pk2(a[8*s+4],a[8*s+5]),pk2(a[8*s+6],a[8*s+7])}; }
DI void hedgehog(const f32x16& p, float r, unsigned (&o)[9]){
  float mx=fmaxf(fmaxf(p[0],p[1]),p[2]), mn=fminf(fminf(p[0],p[1]),p[2]);
  #pragma unroll
  for(int i=3;i<9;i+=2){ mx=fmaxf(fmaxf(mx,p[i]),p[i+1]); mn=fminf(fminf(mn,p[i]),p[i+1]); }
  mx=xhalf_max(mx); mn=xhalf_min(mn);
  const float L=1.4426950408889634f*r; const f32x2 LL={L,-L}, ab={-mx*L,mn*L};
  f32x2 e[9], s={0.f,0.f};
  #pragma unroll
  for(int i=0;i<9;i++){ const f32x2 t=__builtin_elementwise_fma(f32x2{p[i],p[i]},LL,ab); e[i]=f32x2{__builtin_amdgcn_exp2f(t[0]),__builtin_amdgcn_exp2f(t[1])}; s= i? s+e[i] : e[i]; }
  s=f32x2{__builtin_amdgcn_rcpf(xhalf_sum(s[0])),__builtin_amdgcn_rcpf(xhalf_sum(s[1]))};
  #pragma unroll
  for(int i=0;i<9;i++){ const f32x2 t=e[i]*s; o[i]=pk2(t[0],t[1]); }
}
template<bool P> DI void mv32(f32x16& d, const u32x4& a, const u32x4& b){
  if constexpr (P) asm volatile("s_nop 1\n\tv_mfma_f32_32x32x16_bf16 %0, %1, %2, %0" : "+v"(d) : "v"(a), "v"(b)); else asm volatile("v_mfma_f32_32x32x16_bf16 %0, %1, %2, %0" : "+v"(d) : "v"(a), "v"(b)); }
DI void mz32(f32x16& d, const u32x4& a, const u32x4& b){ asm volatile("s_nop 1\n\tv_mfma_f32_32x32x16_bf16 %0, %1, %2, 0" : "=&v"(d) : "v"(a), "v"(b)); }
#define MPAD(a,b) asm volatile("s_nop 7\n\ts_nop 3" : "+v"(a), "+v"(b))
template<int CTRL> DI float dpp_add(float v){ return v+__builtin_bit_cast(float,__builtin_amdgcn_update_dpp(0,__builtin_bit_cast(int,v),CTRL,0xf,0xf,true)); }
DI float dot2(unsigned a,unsigned b,float c){ return __builtin_amdgcn_fdot2_f32_bf16(__builtin_bit_cast(bf16x2,a),__builtin_bit_cast(bf16x2,b),c,false); }
constexpr int K1_RING=73728, K1_SLOT=13824, K1_PAIR=28672, K1_WPHI=K1_RING+2*K1_PAIR+K1_SLOT+2048, K1_STAT=49152, K_LDS=K1_RING+3*K1_PAIR;
constexpr int k1_slot(int s){ return (s>>1)*K1_PAIR+(s&1)*K1_SLOT; }
DI void mv16(f32x4& d, const u32x4& a, const u32x4& b){ asm volatile("v_mfma_f32_16x16x32_bf16 %0, %1, %2, %0" : "+v"(d) : "v"(a), "v"(b)); }
#define SB() __builtin_amdgcn_sched_barrier(0)
constexpr int P1_SLOT=24576, P1_REG=12288;
constexpr int P2_WREG=6656, P2_SLOT=4*P2_WREG;
constexpr int FR_SLAB=53248, FR_KVB=FR_SLAB+576*160;
#define MAP 1
#define CODE_BYTES 47000
#define WQW 60
#define NOFW 0
#define SLA 4
#define SLB 12
#define GAS __attribute__((address_space(1)))
struct P1 {
  u32x4 x[18][4];
  f32x4 acc[2][2][4];
  float tz[2][4][4], tp[2][4][4];
  u32x2 flo[3][2], fhi[3][2];
  unsigned pk[2][4];
  float c1[4], ir2[4];
  unsigned bA[3], bB[3];
  unsigned voff, stoff, vl, vwarm;
  u32x4 srdw, srdact, srdx;
  unsigned m0base, so_cur, so_next, st_prev;
};
struct P2 {
  f32x4 acc[9][4];
  u32x2 flo[6], fhi[6];
  u32x4 bf[2][4], bt[4][4];
  unsigned ba[3][5], bb[3];
  unsigned vw[5], va;
  u32x4 srdw, srda, srdp;
  unsigned m0w, sowb, soab, sow, soa, kk;
  u64 tab;
  GAS unsigned* myflag; GAS unsigned* pflag; unsigned pv, tag, vtag;
  unsigned r[4], vl, ownl, pb[2], ownw[2];
};
DI void bstore128_sc1(u32x4 v, unsigned voff, u32x4 srd, unsigned soff){
  asm volatile("buffer_store_dwordx4 %0, %1, %2, %3 offen sc1\n\ts_nop 1" :: "v"(v), "v"(voff), "s"(srd), "s"(soff) : "memory");
}
DI void bstore64_sc1(u32x2 v, unsigned voff, u32x4 srd, unsigned soff){
  asm volatile("buffer_store_dwordx2 %0, %1, %2, %3 offen sc1" :: "v"(v), "v"(voff), "s"(srd), "s"(soff) : "memory");
}
DI void bstore32_sc1(unsigned v, unsigned voff, u32x4 srd, unsigned soff){
  asm volatile("buffer_store_dword %0, %1, %2, %3 offen sc1" :: "v"(v), "v"(voff), "s"(srd), "s"(soff) : "memory");
}
template<int OFF> DI void glds_off_sc1(unsigned voff, u32x4 srd, unsigned soff){
  asm volatile("buffer_load_dwordx4 %0, %1, %2 offen offset:%3 sc1 lds" :: "v"(voff), "s"(srd), "s"(soff), "i"(OFF) : "memory");
}
template<int OFF> DI void set_m0(unsigned base){ asm volatile("s_add_u32 m0, %0, %1" :: "s"(base), "i"(OFF) : "scc", "memory"); }
template<int OFF> DI void glds_half(unsigned voff, u32x4 srd, unsigned soff){
  asm volatile("buffer_load_dwordx4 %0, %1, %2 offen offset:%3 lds" :: "v"(voff), "s"(srd), "s"(soff), "i"(OFF) : "memory");
}
template<int OFF> DI void ldsread128a(u32x4& d, unsigned addr){ asm volatile("ds_read_b128 %0, %1 offset:%2" : "=a"(d) : "v"(addr), "i"(OFF)); }
template<int OFF,bool SC1> DI void bload128(u32x4& d, unsigned voff, u32x4 srd, unsigned soff){
  if constexpr (SC1) asm volatile("buffer_load_dwordx4 %0, %1, %2, %3 offen offset:%4 sc1" : "=v"(d) : "v"(voff), "s"(srd), "s"(soff), "i"(OFF) : "memory");
  else asm volatile("buffer_load_dwordx4 %0, %1, %2, %3 offen offset:%4" : "=v"(d) : "v"(voff), "s"(srd), "s"(soff), "i"(OFF) : "memory");
}
template<int OFF,bool SC1> DI void bload64(u32x2& d, unsigned voff, u32x4 srd, unsigned soff){
  if constexpr (SC1) asm volatile("buffer_load_dwordx2 %0, %1, %2, %3 offen offset:%4 sc1" : "=v"(d) : "v"(voff), "s"(srd), "s"(soff), "i"(OFF) : "memory");
  else asm volatile("buffer_load_dwordx2 %0, %1, %2, %3 offen offset:%4" : "=v"(d) : "v"(voff), "s"(srd), "s"(soff), "i"(OFF) : "memory");
}
template<int OFF,bool SC1> DI void bload32(unsigned& d, unsigned voff, u32x4 srd, unsigned soff){
  if constexpr (SC1) asm volatile("buffer_load_dword %0, %1, %2, %3 offen offset:%4 sc1" : "=v"(d) : "v"(voff), "s"(srd), "s"(soff), "i"(OFF) : "memory");
  else asm volatile("buffer_load_dword %0, %1, %2, %3 offen offset:%4" : "=v"(d) : "v"(voff), "s"(srd), "s"(soff), "i"(OFF) : "memory");
}
DI void flag_store(GAS unsigned* p, unsigned v){
  asm volatile("s_mov_b64 exec, 1\n\tglobal_store_dword %0, %1, %2 sc1\n\ts_mov_b64 exec, -1" :: "v"(0u), "v"(v), "s"(p) : "memory");
}
DI void flag_load(unsigned& d, GAS unsigned* p){ asm volatile("global_load_dword %0, %1, %2 sc1" : "=v"(d) : "v"(0u), "s"(p) : "memory"); }
DI void flag_check(unsigned& v, GAS unsigned* p, unsigned need){
  unsigned s_, n_;
  asm volatile("s_nop 0\n\tv_readfirstlane_b32 %0, %2\n\ts_cmp_eq_u32 %0, %5\n\ts_cbranch_scc1 .Ldone%=\n\ts_mov_b32 %1, 0x100000\n"
               ".Lspin%=:\n\ts_sleep 2\n\tglobal_load_dword %2, %3, %4 sc1\n\ts_waitcnt vmcnt(0)\n\tv_readfirstlane_b32 %0, %2\n\ts_cmp_eq_u32 %0, %5\n\ts_cbranch_scc1 .Ldone%=\n\t"
               "s_sub_u32 %1, %1, 1\n\ts_cbranch_scc0 .Lspin%=\n.Ldone%=:"
               : "=&s"(s_), "=&s"(n_), "+&v"(v) : "v"(0u), "s"(p), "s"(need) : "scc", "memory");
}
DI void poll_all(GAS unsigned* p, unsigned voff, unsigned skip, unsigned tag){
  unsigned v_, n_;
  asm volatile("s_cmp_lg_u32 %4, 0\n\ts_cbranch_scc1 .Ldone%=\n\ts_mov_b32 %1, 0x100000\n"
               ".Lspin%=:\n\tglobal_load_dword %0, %2, %3 sc1\n\ts_waitcnt vmcnt(0)\n\tv_cmp_ne_u32 vcc, %5, %0\n\ts_cbranch_vccz .Ldone%=\n\t"
               "s_sleep 1\n\ts_sub_u32 %1, %1, 1\n\ts_cbranch_scc0 .Lspin%=\n.Ldone%=:"
               : "=&v"(v_), "=&s"(n_) : "v"(voff), "s"(p), "s"(skip), "s"(tag) : "scc", "vcc", "memory");
}
template<int Z,bool AG> DI void p1_mfma(f32x4& acc, const u32x4& a, const u32x4& x){
  if constexpr (Z==2)  asm volatile("v_mfma_f32_16x16x32_bf16 %0, %1, %2, 0" : "=&v"(acc) : "v"(a), "a"(x));
  if constexpr (Z==1 && AG)  asm volatile("v_mfma_f32_16x16x32_bf16 %0, %1, %2, 0" : "+v"(acc) : "v"(a), "a"(x));
  if constexpr (!Z && AG) asm volatile("v_mfma_f32_16x16x32_bf16 %0, %1, %2, %0" : "+v"(acc) : "v"(a), "a"(x));
  if constexpr (!AG)      asm volatile("v_mfma_f32_16x16x32_bf16 %0, %1, %2, %0" : "+v"(acc) : "v"(a), "v"(x));
}
template<int TL,int I> DI void p1_dr(P1& c){
  constexpr int tt=(I>>2)&3, r=I&3;
  if constexpr (I<16) vmul(c.tz[TL][tt][r], c.acc[TL][0][tt][r], c.c1[tt]); else vmul(c.tp[TL][tt][r], c.acc[TL][0][tt][r], c.acc[TL][1][tt][r]);
}
constexpr int dr_cum(int G){ int n=0; for(int g=0;g<G;g++){ const int m=g&7; n+= m==6?0 : (m&1)?1:2; if(g==1||g==9) n++; } return n; }
template<int TL,int LO,int HI> DI void p1_drs(P1& c){ if constexpr (LO<HI){ p1_dr<TL,LO>(c); p1_drs<TL,LO+1,HI>(c);} }
template<int TT,int HF,int G> DI void p1_grp(P1& c){
  float* z=c.tz[HF][TT]; float* p=c.tp[HF][TT];
  if constexpr (G==0) vexp(z[0]); if constexpr (G==2) vexp(z[1]); if constexpr (G==4) vexp(z[2]); if constexpr (G==8) vexp(z[3]);
  if constexpr (G==5) vfma(z[0],z[0],c.ir2[TT],c.ir2[TT]); if constexpr (G==7) vfma(z[1],z[1],c.ir2[TT],c.ir2[TT]);
  if constexpr (G==9) vfma(z[2],z[2],c.ir2[TT],c.ir2[TT]); if constexpr (G==13) vfma(z[3],z[3],c.ir2[TT],c.ir2[TT]);
  if constexpr (G==10) vrcp(z[0]); if constexpr (G==12) vrcp(z[1]); if constexpr (G==16) vrcp(z[2]); if constexpr (G==18) vrcp(z[3]);
  if constexpr (G==15) vmul(p[0],p[0],z[0]); if constexpr (G==17) vmul(p[1],p[1],z[1]); if constexpr (G==21) vmul(p[2],p[2],z[2]); if constexpr (G==23) vmul(p[3],p[3],z[3]);
}
template<int TT,int HF,int G> DI void p1_cvt(P1& c){ float* p=c.tp[HF][TT];
  if constexpr (G==1) vcvtpk(c.pk[TT&1][2*HF],p[0],p[1]); if constexpr (G==3) vcvtpk(c.pk[TT&1][2*HF+1],p[2],p[3]); }
template<int TT> DI void p1_store(P1& c){ const unsigned* k=c.pk[TT&1]; asm volatile("buffer_store_dwordx4 %0, %1, %2, %3 offen sc1" :: "v"(u32x4{k[0],k[1],k[2],k[3]}), "v"(c.stoff), "s"(c.srdact), "s"(c.st_prev+TT*1024) : "memory"); }
template<int SL> DI void p2_issue(P2& d, int j){
  if(j==0) { set_m0<SL*P2_SLOT>(d.m0w); asm volatile("s_nop 0"); glds_off<0>(d.vw[0],d.srdw,d.sow); }
  if(j==1) glds_off<1024>(d.vw[1],d.srdw,d.sow);
  if(j==2) glds_off<2048>(d.vw[2],d.srdw,d.sow);
  if(j==3) { glds_off<3072>(d.vw[3],d.srdw,d.sow); set_m0<SL*P2_SLOT+4096>(d.m0w); }
  if(j==4) glds_half<0>(d.vw[4],d.srdw,d.sow);
  if(j==5) glds_off_sc1<512>(d.va,d.srda,d.soa);
  if(j==6) glds_off_sc1<1536>(d.va,d.srda,d.soa);
}
DI void p2_next(P2& d){ asm volatile("s_and_b32 %0, %3, 31\n\ts_lshr_b64 %2, %2, 5\n\ts_mul_i32 %1, %0, 0x9000\n\ts_lshl_b32 %0, %0, 13\n\ts_add_u32 %1, %1, %4\n\ts_add_u32 %0, %0, %5"
   : "=&s"(d.soa), "=&s"(d.sow), "+s"(d.tab) : "s"((unsigned)d.tab), "s"(d.sowb), "s"(d.soab) : "scc"); }
template<int ST,int PC> DI void p1_glds(P1& c){ constexpr int SL=(ST+5)%6; const unsigned so=(ST==0? c.so_cur : c.so_next) + (unsigned)((96*SL+8*PC)*9216-(PC&3)*1024); glds_off<(PC&3)*1024>(c.voff, c.srdw, so); }
template<int K,int TT> DI void p1_xload(P1& c){
  if constexpr (K<16) asm volatile("buffer_load_dwordx4 %0, %1, %2, %3 offen offset:%4 sc1" : "=a"(c.x[K][TT]) : "v"(c.vl), "s"(c.srdx), "s"((TT*18+(K&~3))*1024), "i"((K&3)*1024));
  else asm volatile("buffer_load_dwordx4 %0, %1, %2, %3 offen offset:%4 sc1" : "=v"(c.x[K][TT]) : "v"(c.vl), "s"(c.srdx), "s"((TT*18+(K&~3))*1024), "i"((K&3)*1024)); }
template<int ST,int TL,int KK,int M,int MODE> DI void p1_gap(P1& c, P2& d){
  constexpr bool FULL=MODE<2;
  constexpr int G=8*KK+M, SG=2*ST+TL, K=3*ST+KK;
  { const u32x4 a=cat(c.flo[KK][M>>2],c.fhi[KK][M>>2]); p1_mfma<(K==0? (MODE==0?2:1):0),(K<16)>(c.acc[TL][M>>2][M&3], a, c.x[K][M&3]); }
  if constexpr (M&1) {
    constexpr int N=(KK+2)%3; constexpr bool same=KK==0, toB= !same && TL==0 && FULL; constexpr int S2=(same||toB)? ST : (ST+1)%6;
    constexpr int IMM=(S2&1)*P1_SLOT+(M>>2)*P1_REG+N*4096+((M>>1)&1)*2048;
    const unsigned b=((same&&TL)||toB)? c.bB[S2>>1] : c.bA[S2>>1];
    if constexpr (FULL || ST<5 || same) { if constexpr ((M>>1)&1) trread<IMM>(c.fhi[N][M>>2], b); else trread<IMM>(c.flo[N][M>>2], b); }
  }
  if constexpr (FULL) {
    constexpr int PC=3*TL+KK;
    if constexpr (MODE==1 || ST>0) {
      if constexpr (M==4 && (PC==0||PC==4)) set_m0<((ST+5)%6)*P1_SLOT+PC*1024>(c.m0base);
      if constexpr (M==6) p1_glds<ST,PC>(c); }
    if constexpr (SG==11||(SG==0&&MODE==1)) p1_drs<(SG==0),dr_cum(G),dr_cum(G+1)>(c);
    if constexpr (MODE==1) {
      if constexpr (SG>=1 && SG<=8) p1_grp<((SG-1)>>1),((SG-1)&1),G>(c);
      if constexpr (SG>=2 && SG<=9) p1_cvt<((SG-2)>>1),((SG-2)&1),G>(c);
      if constexpr (G==20 && (SG==3||SG==5||SG==7||SG==9)) p1_store<(SG-3)/2>(c);
    } else if constexpr (MODE==0) {
      if constexpr (ST<4 && (M==0||M==2)) p1_xload<3*(ST+2)+(PC>>1),2*(PC&1)+(M>>1)>(c);
      if constexpr (G==20 && SG==9) asm volatile("buffer_load_dword %0, %0, %1, 0 offen" : "+v"(c.vwarm) : "s"(d.srdw) : "memory");
    }
  } else {
    if constexpr (ST==0) { p1_drs<1,dr_cum(G),dr_cum(G+1)>(c);
      if constexpr (M==1 && KK!=1) set_m0<5*P1_SLOT+KK*2048>(c.m0base);
      if constexpr (M==2) p1_glds<0,2*KK>(c); if constexpr (M==6) p1_glds<0,2*KK+1>(c); }
    if constexpr (ST>=1 && ST<=4) { p1_grp<ST-1,0,G>(c); p1_grp<ST-1,1,(G^1)>(c); }
    if constexpr (ST>=2) { p1_cvt<ST-2,0,G>(c); p1_cvt<ST-2,1,(G^1)>(c); }
    if constexpr (ST>=2) { constexpr int j= G==6?0: G==7?1: G==14?2: G==15?3: G==20?4: G==22?5: G==23?6: -1; if constexpr (j>=0) p2_issue<ST-2>(d,j);
      if constexpr (G==21) p1_store<ST-2>(c); if constexpr (G==23) p2_next(d); }
  }
}
template<int ST,int TL,int KK,int FULL> DI void p1_step(P1& c, P2& d){
  constexpr bool LAST=FULL==2 && ST==5;
  WAITL(LAST&&KK==2? 2:6);
  p1_gap<ST,TL,KK,0,FULL>(c,d); p1_gap<ST,TL,KK,1,FULL>(c,d); p1_gap<ST,TL,KK,2,FULL>(c,d); p1_gap<ST,TL,KK,3,FULL>(c,d);
  WAITL(LAST&&KK==2? 0: LAST&&KK==1? 4:6);
  p1_gap<ST,TL,KK,4,FULL>(c,d); p1_gap<ST,TL,KK,5,FULL>(c,d); p1_gap<ST,TL,KK,6,FULL>(c,d); p1_gap<ST,TL,KK,7,FULL>(c,d);
}
template<int ST,int TL,int FULL> DI void p1_section(P1& c, P2& d){ p1_step<ST,TL,0,FULL>(c,d); p1_step<ST,TL,1,FULL>(c,d); p1_step<ST,TL,2,FULL>(c,d); }
template<int ST,int MODE> DI void p1_stage(P1& c, P2& d, unsigned bA_next){
  if constexpr (MODE==2 && ST==2) flag_load(d.pv,d.pflag);
  p1_section<ST,0,MODE>(c,d);
  if constexpr (ST==5 && MODE==1) {
    #pragma unroll
    for(int k=0;k<3;k++) c.bA[k]=bA_next+k*2*P1_SLOT; }
  if constexpr (MODE<2) p1_section<ST,1,MODE>(c,d);
  if constexpr (MODE==0) WAITV(ST==0?12: ST<4?18: ST==4?7:19);
  if constexpr (MODE==1) WAITV(ST<2?19: ST==2?20: ST<5?21:20);
  if constexpr (MODE==2) { if constexpr (ST==0) WAITV(19); if constexpr (ST==1) WAITV(12); if constexpr (ST==2) { WAITV(8); flag_check(d.pv,d.pflag,d.tag); } if constexpr (ST==5) WAITV(16); }
  BAR();
}
template<int MODE> DI void p1_unit(P1& c, P2& d, unsigned bn){ p1_stage<0,MODE>(c,d,bn); p1_stage<1,MODE>(c,d,bn); p1_stage<2,MODE>(c,d,bn); p1_stage<3,MODE>(c,d,bn); p1_stage<4,MODE>(c,d,bn); p1_stage<5,MODE>(c,d,bn); }
template<int I> DI void p1_fin(P1& c){
  if constexpr (I<108) { constexpr int tt=I/27, k=I%27, r=k&3; f32x4 &g=c.acc[0][0][tt], &u=c.acc[0][1][tt]; float t;
    if constexpr (k<4) { vmul(t,g[r],u[r]); u[r]=t; }
    else if constexpr (k<8) { vmul(t,g[r],c.c1[tt]); g[r]=t; }
    else if constexpr (k<12) { t=g[r]; vexp(t); g[r]=t; }
    else if constexpr (k<16) { vfma(t,g[r],c.ir2[tt],c.ir2[tt]); g[r]=t; }
    else if constexpr (k<20) { t=g[r]; vrcp(t); g[r]=t; }
    else if constexpr (k<24) { vmul(t,u[r],g[r]); u[r]=t; }
    else if constexpr (k<26) vcvtpk(c.pk[tt&1][k-24],u[2*(k-24)],u[2*(k-24)+1]);
    else bstore64_sc1(u32x2{c.pk[tt&1][0],c.pk[tt&1][1]}, c.stoff, c.srdact, c.st_prev+tt*1024); }
}
template<bool Z> DI void p2_mfma(f32x4& acc, const u32x4& a, const u32x4& b){
  if constexpr (Z) asm volatile("v_mfma_f32_16x16x32_bf16 %0, %1, %2, 0" : "=&v"(acc) : "v"(a), "a"(b));
  else asm volatile("v_mfma_f32_16x16x32_bf16 %0, %1, %2, %0" : "+v"(acc) : "v"(a), "a"(b)); }
template<bool E,int I> DI void p2_fin(P1& e){ if constexpr (E) { p1_fin<3*I>(e); p1_fin<3*I+1>(e); p1_fin<3*I+2>(e); } }
template<int P,int J,bool Z> DI void p2_step(P2& c, P1& e){
  constexpr int PAR=P&1, ST=PAR*9+J, CUR=ST%6, NX=(ST+3)%6; constexpr int JN=(J+3)%9; constexpr int PN=(J+3<9)?P:(P+1)%6;
  constexpr int N = J<3?4: J==3?5: J==4?6: J<7?7: J==7?6:5;
  constexpr int IMM=(PN&1)*P2_SLOT+(JN<8?(JN>>2)*1024:0); constexpr int BI=JN<8?(JN&3):4;
  constexpr int P1n=(P+1)%6, PF=(P+5)%6;
  WAITL(N);
  u32x4 a=cat(c.flo[CUR],c.fhi[CUR]);
  p2_mfma<Z>(c.acc[J][0],a,c.bf[PAR][0]);
  trread<IMM>(c.flo[NX], c.ba[PN>>1][BI]);
  p2_fin<Z,4*J>(e);
  p2_mfma<Z>(c.acc[J][1],a,c.bf[PAR][1]);
  trread<IMM+2*P2_WREG>(c.fhi[NX], c.ba[PN>>1][BI]);
  p2_fin<Z,4*J+1>(e);
  p2_mfma<Z>(c.acc[J][2],a,c.bf[PAR][2]);
  if constexpr (J>=2 && J<=5) ldsread128a<(P1n&1)*P2_SLOT+((J-2)>>1)*P2_WREG+((J-2)&1)*1024>(c.bf[PAR^1][J-2], c.bb[P1n>>1]);
  if constexpr (J==0) set_m0<PF*P2_SLOT>(c.m0w);
  if constexpr (J==7) asm volatile("s_bfe_u32 %0, %1, %2" : "=s"(c.kk) : "s"((unsigned)c.tab), "i"(5<<16) : "scc");
  if constexpr (J==8) asm volatile("s_mul_i32 %0, %1, 0x9000" : "=s"(c.sow) : "s"(c.kk));
  p2_fin<Z,4*J+2>(e);
  p2_mfma<Z>(c.acc[J][3],a,c.bf[PAR][3]);
  if constexpr (J<=3) glds_off<J*1024>(c.vw[J], c.srdw, c.sow);
  if constexpr (J==3) set_m0<PF*P2_SLOT+4096>(c.m0w);
  if constexpr (J==4) glds_half<0>(c.vw[4], c.srdw, c.sow);
  if constexpr (J==5) glds_off_sc1<512>(c.va, c.srda, c.soa);
  if constexpr (J==6) glds_off_sc1<1536>(c.va, c.srda, c.soa);
  if constexpr (J==7) { asm volatile("s_lshl_b32 %0, %1, 13\n\ts_lshr_b64 %2, %2, 5" : "=&s"(c.soa), "+s"(c.kk), "+s"(c.tab) :: "scc"); }
  if constexpr (J==8) { asm volatile("s_add_u32 %0, %0, %2\n\ts_add_u32 %1, %1, %3" : "+s"(c.sow), "+s"(c.soa) : "s"(c.sowb), "s"(c.soab) : "scc"); }
  p2_fin<Z,4*J+3>(e);
}
template<int P,bool Z> DI void p2_stage(P2& c, P1& e){
  if constexpr (P==3) flag_store(c.myflag+1,c.vtag);
  if constexpr (P==5) flag_load(c.pv,c.pflag+1);
  p2_step<P,0,Z>(c,e); p2_step<P,1,Z>(c,e); p2_step<P,2,Z>(c,e); p2_step<P,3,Z>(c,e); p2_step<P,4,Z>(c,e); p2_step<P,5,Z>(c,e); p2_step<P,6,Z>(c,e); p2_step<P,7,Z>(c,e); p2_step<P,8,Z>(c,e);
  if constexpr (Z) WAITV(26); else if constexpr (P==2) WAITV(14); else if constexpr (P==5) { WAITV(7); flag_check(c.pv,c.pflag+1,c.tag); } else WAITV(21);
  BAR();
}
template<int J,int U,int K> DI void p2_dump(P2& c){
  if constexpr (K<4) vcvtpk(c.r[K],c.acc[J][2*U+(K>>1)][2*(K&1)],c.acc[J][2*U+(K>>1)][2*(K&1)+1]);
  else asm volatile("buffer_store_dwordx4 %0, %1, %2, %3 offen sc1\n\tds_write_b128 %4, %0 offset:%5\n\ts_nop 0"
                    :: "v"(u32x4{c.r[0],c.r[1],c.r[2],c.r[3]}), "v"(c.vl), "s"(c.srdp), "s"(c.pb[U]+J*1024), "v"(c.ownw[U]), "i"(J*1024) : "memory");
}
template<int GI,int I> DI void p2_dumpq(P2& c){ if constexpr (GI>=0 && I>=0 && I<30) p2_dump<3*GI+I/10,(I/5)&1,I%5>(c); }
template<int GI,int S,int JJ> DI void p2_tstep(P2& c){
  constexpr int n=(GI*5+S)*3+JJ, CUR=(n+3)%6, NX=n%6, J=3*GI+JJ;
  constexpr int SN=S<4? S+1:0, JN=S<4? J : J+3, PN=SN+1;
  constexpr int IMM=(PN&1)*P2_SLOT+(JN<8?(JN>>2)*1024:0); constexpr int BI=JN<8?(JN&3):4;
  constexpr bool RD=n+3<45;
  if constexpr (GI==0) { if constexpr (JJ==0) WAITL(S==0?4:2); if constexpr (JJ==1) WAITL(S==0?6:S<4?8:4); if constexpr (JJ==2) WAITL(S<4?8:4); }
  else WAITL(n<43? 4 : n==43? 2:0);
  const u32x4 a=cat(c.flo[CUR],c.fhi[CUR]);
  const u32x4* b= S==0? c.bf[1] : c.bt[S-1<0?0:S-1];
  p2_mfma<false>(c.acc[J][0],a,b[0]);
  if constexpr (RD) trread<IMM>(c.flo[NX], c.ba[PN>>1][BI]);
  p2_mfma<false>(c.acc[J][1],a,b[1]);
  if constexpr (RD) trread<IMM+2*P2_WREG>(c.fhi[NX], c.ba[PN>>1][BI]);
  p2_mfma<false>(c.acc[J][2],a,b[2]);
  if constexpr (GI==0 && S<4 && JJ<2) ldsread128a<((S+2)&1)*P2_SLOT+JJ*P2_WREG>(c.bt[S][2*JJ], c.bb[(S+2)>>1]);
  p2_dumpq<GI-1,(S*3+JJ-1)*3>(c); p2_dumpq<GI-1,(S*3+JJ-1)*3+1>(c);
  p2_mfma<false>(c.acc[J][3],a,b[3]);
  if constexpr (GI==0 && S<4 && JJ<2) ldsread128a<((S+2)&1)*P2_SLOT+JJ*P2_WREG+1024>(c.bt[S][2*JJ+1], c.bb[(S+2)>>1]);
  p2_dumpq<GI-1,(S*3+JJ-1)*3+2>(c);
}
template<int GI,int S> DI void p2_tstage(P2& c){ p2_tstep<GI,S,0>(c); p2_tstep<GI,S,1>(c); p2_tstep<GI,S,2>(c);
  if constexpr (GI==0 && S<3) { WAITV(14-7*S); BAR(); } }
template<int GI> DI void p2_tgroup(P2& c){ p2_tstage<GI,0>(c); p2_tstage<GI,1>(c); p2_tstage<GI,2>(c); p2_tstage<GI,3>(c); p2_tstage<GI,4>(c); }
template<int I> DI void p2_dump_all(P2& c){ if constexpr (I<30){ p2_dumpq<2,I>(c); p2_dump_all<I+1>(c); } }
DI float row_bcast2(float v){
  auto a=__builtin_amdgcn_permlane32_swap(__float_as_uint(v),__float_as_uint(v),false,false);
  auto b=__builtin_amdgcn_permlane16_swap(a[1],a[1],false,false); return __uint_as_float(b[0]); }
DI float rows_sum(float v){ auto b=__builtin_amdgcn_permlane16_swap(__float_as_uint(v),__float_as_uint(v),false,false); return xhalf_sum(__uint_as_float(b[0])+__uint_as_float(b[1])); }
template<bool LO> DI unsigned slab_voff(int pc, int rr, int cc){
  const int R=6*pc+rr, S=R>>5, g=(R>>2)&3, j=4*((R>>4)&1)+(R&3), a=2*S+(g>>1), hp=g&1;
  const bool lo= LO || a<32; const unsigned row=mad24(18,lo? a : 8*(a-32)+4*hp+(j>>1),lo? 8*hp+j : 16+(j&1));
  return mad24(row,1152,cc*16); }
DI void slab_glds(unsigned m0, unsigned voff, u32x4 srd, unsigned soff){
  asm volatile("s_mov_b32 m0, %0\n\ts_mov_b64 exec, %4\n\tbuffer_load_dwordx4 %1, %2, %3 offen lds\n\ts_mov_b64 exec, -1" :: "s"(m0), "v"(voff), "s"(srd), "s"(soff), "s"(0x07FDFF7FDFF7FDFFull) : "memory"); }

__global__ __launch_bounds__(256,1) void k3(const u16* __restrict__ x, const u16* __restrict__ wqkv, const u16* __restrict__ wphi, const u16* __restrict__ wo, const u16* __restrict__ wg, const u16* __restrict__ wu, const u16* __restrict__ wd,
                                            char* __restrict__ ws, u16* __restrict__ out STAMP_ARG){
  STAMP_DECL STAMPIT(0);
  extern __shared__ __attribute__((aligned(16))) char lds[];
  asm volatile("" :: "s"(x), "s"(wqkv), "s"(wphi), "s"(wo), "s"(wg), "s"(wu), "s"(wd), "s"(ws), "s"(out));
  const int tid=threadIdx.x, lane=tid&63; const int w=rfl(tid>>6);
  const int xq=blockIdx.x&7, i=blockIdx.x>>3, isB=i&1;
  const int xcd=(xq&(8-MAP))|((i>>1)&(MAP-1)), T=(xq&(MAP-1))*(16/MAP)+(i>>1)/MAP;
  const int tb=xq*4+(i&3), cg=i>>2;
  const int me=(T*2+isB)*8+xcd;
  const unsigned ldsb=(unsigned)(size_t)lds;
  GAS unsigned* flags=(GAS unsigned*)(ws+WS_FLAGS);
  const u32x4 srdws=make_srd(ws, WS_SMALL);
  const int g=lane>>4, l15=lane&15, q=l15>>2, p=lane&3;
  P1 c; P2 d;
  unsigned selL=0x3f80u, selH=0x3f800000u; asm volatile("" : "+s"(selL), "+s"(selH));
  const int th=w>>1, fh=w&1;
  c.srdw=make_srd(th? wu:wg, D_*F_*2);
  { const int cc=lane&7; c.voff=(lane>>3)*9216+(((cc>>1)^((lane>>4)&3))*32)+(cc&1)*16; }
  c.m0base=ldsb+th*P1_REG+fh*6*1024;
  const unsigned rowoff=(unsigned)(fh*48)*9216u;
  auto unit_of=[&](int it){ return it<4 ? (isB?5:0)+((it+T)&3) : 4; };
  auto so_of=[&](int it){ return (unsigned)((xcd*9+unit_of(it))*128)+rowoff; };
  #define WST(lo,hi) if(w==1||w==2){ _Pragma("unroll 1") for(int s=lo;s<hi;s++) _Pragma("unroll") for(int j=0;j<12;j++) glds_m0(ldsb+th*P1_REG+s*P1_SLOT+j*1024, c.voff, c.srdw, so_of(0)-rowoff+(unsigned)(96*s+8*j)*9216u); }
  unsigned tag, vtag;
  const int h=lane>>5, blk=(lane>>4)&1; const unsigned vl=lane*16;
  const u32x4 srdo=make_srd(wo, D_*D_*2);
  unsigned sv[8];
  #define SLAB(lo,hi) _Pragma("unroll") for(int it=lo;it<hi;it++) slab_glds(ldsb+FR_SLAB+(w+4*it)*960, sv[it<20? it&3 : it-16], srdo, cg*144+(it<20?(it>>2)*108*1152:0));
  {
    const int tbA=(xq>>1)*8+(i&7), hg=(xq&1)*4+(i>>3), head=4*hg+w, off=2*(w&1);
    const u32x4 srdx=make_srd(x+(size_t)tbA*64*D_, 64*D_*2), srdw=make_srd(wqkv, D_*3*D_*2);
    const unsigned vx0=(lane>>3)*1152+(((lane&7)^(lane>>4))*16), vx1=(vx0^64)+8*1152;
    #pragma unroll
    for(int b=0;b<9;b++){ glds_m0(ldsb+w*2048+b*8192, vx0, srdx, w*16*1152+b*128); glds_m0(ldsb+w*2048+b*8192+1024, vx1, srdx, w*16*1152+b*128); }
    flag_load(vtag, flags+64*128+64);
    unsigned cw;
    { u64 pc; asm volatile("s_getpc_b64 %0" : "=s"(pc)); const u32x4 srdc=make_srd((const void*)(pc&~127ull), CODE_BYTES);
      bload32<0,false>(cw, (lane<13 && w==3)? lane*128 : 0x80000000u, srdc, i*13*128); }
    u32x4 wr0, wr1; const unsigned worw = w*256+blockIdx.x;
    bload128<0,false>(wr0, vl, srdo, worw*1152); bload128<1024,false>(wr1, lane<8? vl:0x80000000u, srdo, worw*1152);
    asm volatile("s_mov_b32 m0, %0\n\ts_nop 0\n\tbuffer_load_dwordx4 %1, %3, 0 offen lds\n\tbuffer_load_dword %2, %3, 0 offen offset:640 lds"
                 :: "s"(ldsb+K1_WPHI+3712+w*1024), "v"(lane<40? vl:0x80000000u), "v"(lane*4), "s"(make_srd(wphi+head*324, 648)) : "memory");
    unsigned wq[2];
    { const unsigned r15=divm<69,10>(lane), c15=lane-r15*15; const unsigned v=mad24(r15,3456,mad24(divm<52,8>(c15),512,c15*128))|(lane<WQW? 0:0x80000000u);
      bload32<0,false>(wq[0], v, srdw, (i*18+w*4)*3456+(xq&1)*512); bload32<0,false>(wq[1], (w==0 && lane<30)? v:0x80000000u, srdw, (i*18+16)*3456+(xq&1)*512); }
    unsigned vw[7];
    #pragma unroll
    for(int k=0;k<7;k++){ const unsigned ck=(w+4*k)*64+lane, row=divm<2428,16>(ck), cc=ck-__umul24(row,27); vw[k]=mad24(row,3456,mad24(divm<57,9>(cc),1008,cc*16)); }
    vw[6]|= (unsigned)(w+1)<<29&0x80000000u;
    const unsigned sw0=hg*144, mw0=ldsb+K1_RING+w*1024;
    #define A_W(PR,LO,HI) _Pragma("unroll") for(int k=LO;k<HI;k++) glds_m0(mw0+(PR)*K1_PAIR+k*4096, vw[k], srdw, sw0+(PR)*64*3456);
    STAMPIT(28);
    A_W(0,0,7)
    unsigned bx[4], bw[2];
    { const int row=lane&31;
      #pragma unroll
      for(int e=0;e<4;e++) bx[e]=ldsb+row*128+(((2*e+h)^((row>>1)&7))*16);
      const int hoff=36*w-2*off;
      #pragma unroll
      for(int t=0;t<2;t++){ unsigned gi=8*t+4*blk+p; gi=gi>14?14:gi; bw[t]=ldsb+K1_RING+mad24(8*h+q,432,mad24(divm<52,8>(gi),104,8*gi))+hoff; } }
    unsigned warm[2], wdst[2];
    { const unsigned r9=divm<57,9>(lane); warm[0]=mad24(r9,9216-1152,lane*128)|(lane<54? 0:0x80000000u); warm[1]= fh? 0x80000000u : warm[0]+6*9216; }
    f32x16 acc[2][2]={}; f32x4 cs4={}; float s1[2]={0,0}, s2[2]={0,0};
    u32x4 sel[2];
    #pragma unroll
    for(int t=0;t<2;t++){ const unsigned v= l15==2*t+(g&1)? 0x3f803f80u:0u; sel[t]=u32x4{v,v,v,v}; }
    unsigned wa[8];
    #pragma unroll
    for(int k=0;k<4;k++){ const unsigned idx=lane+64*k, dd=divm<683,13>(idx), fr=(idx-dd*12)*2, reg=(fr&3)+4*(fr>>3); const unsigned o=mad24(dd,36,((fr>>2)&1)*18+reg*2);
      wa[2*k]=ldsb+K1_WPHI+3712+w*1024+((reg<9 && dd<18)? o:648); wa[2*k+1]=ldsb+K1_WPHI+3712+w*1024+((reg+1<9 && dd<18)? o+2:648); }
    asm volatile("" : "+v"(acc[0][0]), "+v"(acc[0][1]), "+v"(acc[1][0]), "+v"(acc[1][1]), "+v"(cs4), "+v"(sel[0]), "+v"(sel[1]), "+v"(bx[0]), "+v"(bx[1]), "+v"(bx[2]), "+v"(bx[3]), "+v"(bw[0]), "+v"(bw[1]), "+v"(warm[0]), "+v"(warm[1]));
    asm volatile("" : "+v"(wa[0]), "+v"(wa[1]), "+v"(wa[2]), "+v"(wa[3]), "+v"(wa[4]), "+v"(wa[5]), "+v"(wa[6]), "+v"(wa[7]));
    A_W(1,0,7)
    STAMPIT(29);
    asm volatile("s_waitcnt vmcnt(14)" : "+v"(vtag), "+v"(wr0), "+v"(wr1), "+v"(cw), "+v"(wq[0]), "+v"(wq[1]) :: "memory");
    {
      const unsigned a=ldsb+K1_WPHI+w*912+lane*4; unsigned v[4];
      #pragma unroll
      for(int k=0;k<4;k++) v[k]=*LDSP(u16,wa[2*k])|((unsigned)*LDSP(u16,wa[2*k+1])<<16);
      *LDSP(unsigned,a)=v[0]; *LDSP(unsigned,a+256)=v[1]; *LDSP(unsigned,a+512)=v[2]; if(lane<36) *LDSP(unsigned,a+768)=v[3]; }
    tag=rfl(vtag)*0x9E3779B1u+0x7F4A7C15u; vtag=tag; asm volatile("" : "+v"(vtag));
    { float s=0.f;
      #pragma unroll
      for(int k=0;k<4;k++){ s=dot2(wr0[k],0x3f803f80u,s); s=dot2(wr1[k],0x3f803f80u,s); }
      s=dpp_add<0xB1>(s); s=dpp_add<0x4E>(s); s=dpp_add<0x141>(s); s=dpp_add<0x140>(s); s=rows_sum(s);
      const unsigned hi=pk2(s,0.f)&0xffffu; const unsigned lo=pk2(s-lo2f(hi),0.f);
      const unsigned hd=(worw*3641u)>>16, dd=worw-hd*18;
      const unsigned a= dd<16? hd : 32+(hd>>3), hp= dd<16? dd>>3 : (hd>>2)&1, j= dd<16? dd&7 : 2*(hd&3)+(dd-16);
      bstore64_sc1(u32x2{hi|(lo<<16),vtag}, (lane || worw>=576)? 0x80000000u:0u, srdws, WS_WOROW+(32*(a>>1)+16*(j>>2)+4*(2*(a&1)+hp)+(j&3))*8); }
    u32x4 wq0,wq1,wk0,wk1;
    { const unsigned wb=ldsb+K1_WPHI+w*912+blk*32+p*8;
      auto wrow=[&](int dd){ return wb+((unsigned)dd<18u? dd:18)*48; };
      const int R=4*h+q;
      wk0=trfrag(wrow(R>=4? R-4-off:99),wrow(R+4-off)); wk1=trfrag(wrow(R+12-off),wrow(99));
      wq0=trfrag(wrow(R-off),wrow(R+8-off)); wq1=trfrag(wrow(R<4? R+16-off:99),wrow(99)); }
    A_W(2,0,3)
    asm volatile("s_waitcnt vmcnt(11) lgkmcnt(0)\n\ts_barrier" : "+v"(wq0), "+v"(wq1), "+v"(wk0), "+v"(wk1) :: "memory");
    STAMPIT(22);
    u32x4 xf[2][2][2], st[2]; u32x2 al[2][4], ah[2][4];
    const unsigned bst=ldsb+w*2048+vl;
    #define K1_X(B,E,TT,XB,E2) xf[B][E][TT]=*LDSP(u32x4,bx[2*(E2)+(E)]+(XB)*8192+(TT)*4096);
    #define K1_A(B,I,SL) { al[B][I]=trb(bw[(I)&1]+k1_slot(SL)+((I)>>1)*6912); ah[B][I]=trb(bw[(I)&1]+k1_slot(SL)+((I)>>1)*6912+1728); }
    K1_X(0,0,0,0,0) K1_X(0,0,1,0,0) K1_A(0,0,0) K1_A(0,1,0) K1_X(0,1,0,0,0) K1_X(0,1,1,0,0) K1_A(0,2,0) K1_A(0,3,0) st[0]=*LDSP(u32x4,bst);
    #define MM(D,I,B) SB(); mv32<false>(D,cat(al[cu][I],ah[cu][I]),B); SB();
    #define MC(D,I) SB(); mv16(cs4,sel[(I)&1],cat(al[cu][I],ah[cu][I])); SB();
    #define WL(K) if(cu==0 || (K)<3) glds_m0(mw0+(((j+5)%6)>>1)*K1_PAIR+((cu?0:3)+(K))*4096, vw[(cu?0:3)+(K)], srdw, sw);
    #define SD(K) s1[cu]=dot2(st[cu][K],0x3f803f80u,s1[cu]); s2[cu]=dot2(st[cu][K],st[cu][K],s2[cu]);
    #pragma unroll 1
    for(int s6=0;s6<18;s6+=6){
      #pragma unroll
      for(int j=0;j<6;j++){
        const int s=s6+j; const int cu=j&1, n=cu^1;
        const unsigned sw=sw0+((s+5)>>1)*64*3456;

        MM(acc[0][0],0,xf[cu][0][0])
        WL(0)
        MM(acc[0][1],0,xf[cu][0][1])
        K1_X(n,0,0,(s+1)>>1,n) K1_X(n,0,1,(s+1)>>1,n) SD(0)
        MC(cs[0],0)
        K1_A(n,0,(j+1)%6) SD(1)
        MM(acc[1][0],1,xf[cu][0][0])
        WL(1)
        MM(acc[1][1],1,xf[cu][0][1])
        K1_A(n,1,(j+1)%6) SD(2)
        MC(cs[1],1)
        K1_X(n,1,0,(s+1)>>1,n) K1_X(n,1,1,(s+1)>>1,n) SD(3)
        MM(acc[0][0],2,xf[cu][1][0])
        WL(2)
        MM(acc[0][1],2,xf[cu][1][1])
        K1_A(n,2,(j+1)%6)
        MC(cs[0],2)
        K1_A(n,3,(j+1)%6)
        MM(acc[1][0],3,xf[cu][1][0])
        WL(3)
        MC(cs[1],3)
        st[n]=*LDSP(u32x4,bst+((s+1)>>1)*8192+n*1024);
        MM(acc[1][1],3,xf[cu][1][1])
        asm volatile("s_waitcnt vmcnt(%0)\n\ts_barrier" :: "i"(cu? 10:7) : "memory");
      }
    }
    #undef MM
    #undef SD
    #undef K1_X
    #undef K1_A
    #undef WL
    #undef A_W
    STAMPIT(23);
    asm volatile("s_nop 7" : "+v"(cs4));
    if(lane<16){
      #pragma unroll
      for(int k=0;k<4;k++) *LDSP(float,ldsb+K1_STAT+512+w*256+k*64+lane*4)=cs4[k]; }
    #pragma unroll
    for(int j=0;j<2;j++){ float a=s1[j], b=s2[j];
      a=dpp_add<0xB1>(a); b=dpp_add<0xB1>(b); a=dpp_add<0x4E>(a); b=dpp_add<0x4E>(b); a=dpp_add<0x141>(a); b=dpp_add<0x141>(b);
      const float m=a*(1.f/D_), r=__builtin_amdgcn_rsqf(fmaxf(b*(1.f/D_)-m*m,0.f)+1e-5f);
      *LDSP(f32x2,ldsb+K1_STAT+(w*16+j*8+(lane>>3))*8)=f32x2{-m,r}; }
    asm volatile("s_waitcnt vmcnt(0) lgkmcnt(0)\n\ts_barrier" ::: "memory");
    bload32<0,false>(wdst[0], warm[0], c.srdw, xcd*1152+((T*2+isB)*18+fh*12)*9216+NOFW); bload32<0,false>(wdst[1], warm[1], c.srdw, xcd*1152+((T*2+isB)*18+fh*12)*9216+NOFW);
    asm volatile("" : "+v"(acc[0][0]), "+v"(acc[0][1]), "+v"(acc[1][0]), "+v"(acc[1][1]));
    f32x16 cs[2];
    #pragma unroll
    for(int t=0;t<2;t++)
      #pragma unroll
      for(int k=0;k<4;k++){ const f32x4 v=*LDSP(f32x4,ldsb+K1_STAT+512+w*256+t*128+k*32+h*16); cs[t][4*k]=v[0]; cs[t][4*k+1]=v[1]; cs[t][4*k+2]=v[2]; cs[t][4*k+3]=v[3]; }
    const f32x2 mr[2]={*LDSP(f32x2,ldsb+K1_STAT+(lane&31)*8), *LDSP(f32x2,ldsb+K1_STAT+256+(lane&31)*8)};
    const unsigned PK=ldsb+w*10240, VV=PK+5120+64, KT=VV+3072;
    u32x4 f00[2], f01[2], f10[2]; f32x16 pk[2], pq[2];
    #pragma unroll
    for(int t=0;t<2;t++){
      acc[0][t]=__builtin_elementwise_fma(cs[0],(f32x16)mr[t][0],acc[0][t]); acc[1][t]=__builtin_elementwise_fma(cs[1],(f32x16)mr[t][0],acc[1][t]);
      f00[t]=packfrag(acc[0][t],0); f01[t]=packfrag(acc[0][t],1); f10[t]=packfrag(acc[1][t],0); }
    mz32(pk[0],wk0,f01[0]); mz32(pk[1],wk0,f01[1]); mv32<true>(pk[0],wk1,f10[0]); mv32<true>(pk[1],wk1,f10[1]);
    mz32(pq[0],wq0,f00[0]); mz32(pq[1],wq0,f00[1]); mv32<false>(pq[0],wq1,f01[0]); mv32<false>(pq[1],wq1,f01[1]);
    asm volatile("s_nop 7" : "+v"(pk[0]), "+v"(pk[1]));
    #pragma unroll
    for(int t=0;t<2;t++){
      unsigned ok[9]; hedgehog(pk[t],mr[t][1],ok);
      const int tok=32*t+(lane&31); const float r=mr[t][1]; const f32x16& v=acc[1][t];
      const unsigned pa=PK+tok*80+h*40;
      *LDSP(u32x2,pa)=u32x2{ok[0],ok[1]}; *LDSP(u32x2,pa+8)=u32x2{ok[2],ok[3]}; *LDSP(u32x2,pa+16)=u32x2{ok[4],ok[5]}; *LDSP(u32x2,pa+24)=u32x2{ok[6],ok[7]}; *LDSP(u32x2,pa+32)=u32x2{ok[8],0u};
      const unsigned va=VV+tok*48+h*8;
      *LDSP(u32x2,va)=u32x2{pk2(v[4]*r,v[5]*r),pk2(v[6]*r,v[7]*r)}; *LDSP(u32x2,va+16)=u32x2{pk2(v[8]*r,v[9]*r),pk2(v[10]*r,v[11]*r)};
      *LDSP(u32x2,va+32)= h? u32x2{0x3f803f80u,0x3f803f80u} : u32x2{pk2(v[12]*r,v[13]*r),pk2(v[14]*r,v[15]*r)}; }
    { f32x16 kv0, kv1;
      const unsigned pa=PK+(8*h+q)*80+blk*32+p*8, va=VV+(8*h+q)*48+blk*32+p*8;
      #pragma unroll
      for(int s=0;s<4;s++){ const u32x4 b=trfrag(va+s*768,va+s*768+192), a0=trfrag(pa+s*1280,pa+s*1280+320), a1=trfrag(pa+64+s*1280,pa+64+s*1280+320);
        if(s==0){ mz32(kv0,a0,b); mz32(kv1,a1,b); } else { mv32<false>(kv0,a0,b); mv32<false>(kv1,a1,b); } }
      MPAD(kv0,kv1);
      const int vc=lane&31, r19 = vc==20 ? 18 : vc-off; const unsigned dst=KT+r19*80+8*h;
      if((unsigned)(vc-off)<18u || vc==20){ *LDSP(u32x2,dst)=u32x2{pk2(kv0[0],kv0[1]),pk2(kv0[2],kv0[3])}; *LDSP(u32x2,dst+16)=u32x2{pk2(kv0[4],kv0[5]),pk2(kv0[6],kv0[7])};
        *LDSP(u32x2,dst+32)=u32x2{pk2(kv0[8],kv0[9]),pk2(kv0[10],kv0[11])}; *LDSP(u32x2,dst+48)=u32x2{pk2(kv0[12],kv0[13]),pk2(kv0[14],kv0[15])}; *LDSP(u32x2,dst+64)=u32x2{pk2(kv1[0],kv1[1]),pk2(kv1[2],kv1[3])}; }
      const u32x4 t0=*LDSP(u32x4,KT+vl), t1=*LDSP(u32x4,KT+1024+(vl&511));
      const unsigned so=WS_KVP+(head*32+tbA)*1536;
      bstore128_sc1(t0, vl, srdws, so); bstore128_sc1(t1, h? 0x80000000u:vl, srdws, so+1024);
      bstore32_sc1(__float_as_uint(-(h? mr[1][0]:mr[0][0])), (hg|w)? 0x80000000u : lane*4, srdws, WS_MEANX+tbA*256); }
    STAMPIT(24);
    asm volatile("" : "+v"(pq[0]), "+v"(pq[1]));
    unsigned oq[2][9];
    #pragma unroll
    for(int t=0;t<2;t++) hedgehog(pq[t],mr[t][1],oq[t]);
    asm volatile("s_waitcnt vmcnt(0)" : "+v"(oq[0][8]), "+v"(oq[1][8]), "+v"(wdst[0]), "+v"(wdst[1])); flag_store(flags+(32+head)*128+64+tbA, vtag);
    #pragma unroll
    for(int t=0;t<2;t++){ const unsigned so=WS_FQ+(head*64+tbA*2+t)*2304;
      bstore128_sc1(u32x4{oq[t][0],oq[t][1],oq[t][2],oq[t][3]}, vl, srdws, so); bstore128_sc1(u32x4{oq[t][4],oq[t][5],oq[t][6],oq[t][7]}, vl, srdws, so+1024); bstore32_sc1(oq[t][8], lane*4, srdws, so+2048); }
    STAMPIT(25);
    WAITV(0);
    flag_store(flags+(32+head)*128+96+tbA, vtag);
    STAMPIT(26);
  }
  u32x2 xr[5]; unsigned mx;
  {
    const int head=(i&3)*8+xq, tr=i>>2;
    poll_all(flags+(32+head)*128+64, (lane&31)*4, w, tag);
    BAR();
    STAMPIT(27);
    u32x4 pv[16], q0[2], q1[2]; unsigned q2[2]; u32x2 wr[3];
    { const unsigned vp= (lane&31)<24? h*1536+(lane&31)*16 : 0x80000000u;
      #pragma unroll
      for(int k=0;k<8;k++){ bload128<0,true>(pv[2*k], vp, srdws, WS_KVP+head*49152+w*384+k*6144); bload128<3072,true>(pv[2*k+1], vp, srdws, WS_KVP+head*49152+w*384+k*6144); } }
    bload64<0,true>(wr[0],tid*8,srdws,WS_WOROW); bload64<2048,true>(wr[1],tid*8,srdws,WS_WOROW); bload64<0,true>(wr[2],(tid&63)*8,srdws,WS_WOROW+4096);
    { const int rr10=divm<103,10>(lane), cc10=lane-rr10*10;
      #pragma unroll
      for(int k=0;k<4;k++){ sv[k]=slab_voff<true>(w+4*k,rr10,cc10); sv[4+k]=slab_voff<false>(w+4*(20+k),rr10,cc10); asm volatile("" : "+v"(sv[k]), "+v"(sv[4+k])); } }
    { const int r=lane&31; const unsigned one= r<8? ((r&1)? 0x3f800000u:0x3f80u) : 0u;
      const u32x4 sel={(r>>1)==0?one:0u,(r>>1)==1?one:0u,(r>>1)==2?one:0u,(r>>1)==3?one:0u};
      f32x16 z;
      #pragma unroll
      for(int k=0;k<16;k++){ asm volatile("s_waitcnt vmcnt(%1)" : "+v"(pv[k]) : "i"(18-k)); if(k==0) mz32(z,sel,pv[0]); else mv32<false>(z,sel,pv[k]); }
      asm volatile("s_nop 7\n\ts_nop 3" : "+v"(z));
      const bool zero= w==3 && r>=23;
      if(r<24 || (w==3 && r<28)) *LDSP(u32x2,ldsb+FR_KVB+(24*w+r)*16+8*h)= zero? u32x2{0,0} : u32x2{pk2(z[0],z[1]),pk2(z[2],z[3])}; }
    STAMPIT(1);
    for(int n=0;n<0x100000;n++){
      asm volatile("s_waitcnt vmcnt(0)" : "+v"(wr[0]), "+v"(wr[1]), "+v"(wr[2]));
      if(__builtin_amdgcn_ballot_w64(wr[0][1]!=tag || wr[1][1]!=tag || wr[2][1]!=tag)==0) break;
      __builtin_amdgcn_s_sleep(2);
      bload64<0,true>(wr[0],tid*8,srdws,WS_WOROW); bload64<2048,true>(wr[1],tid*8,srdws,WS_WOROW); bload64<0,true>(wr[2],(tid&63)*8,srdws,WS_WOROW+4096); }
    asm volatile("s_waitcnt vmcnt(0)" : "+v"(wr[0]), "+v"(wr[1]), "+v"(wr[2]));
    { const unsigned a=ldsb+FR_SLAB+tid*160+144; *LDSP(u32x4,a)=u32x4{wr[0][0],0,0,0}; *LDSP(u32x4,a+256*160)=u32x4{wr[1][0],0,0,0}; if(tid<64) *LDSP(u32x4,a+512*160)=u32x4{wr[2][0],0,0,0}; }
    poll_all(flags+(32+head)*128+96+tr*4+w, 0, 0, tag);
    #pragma unroll
    for(int t=0;t<2;t++){ const unsigned so=WS_FQ+(head*64+tr*8+w*2+t)*2304; bload128<0,true>(q0[t],vl,srdws,so); bload128<1024,true>(q1[t],vl,srdws,so); bload32<2048,true>(q2[t],lane*4,srdws,so); }
    SLAB(0,SLA)
    asm volatile("s_waitcnt lgkmcnt(0)\n\ts_barrier" ::: "memory");
    STAMPIT(16);
    const int r=lane&31, hf=(r>>2)&1, reg=(r&3)+4*(r>>3); const int r19 = reg<8 ? 8*hf+reg : reg==10 ? 18 : (reg<10 && !hf) ? 8+reg : 19;
    const unsigned ap=ldsb+FR_KVB+r19*80+h*40;
    const u32x2 b0=*LDSP(u32x2,ap), b1=*LDSP(u32x2,ap+8), b2=*LDSP(u32x2,ap+16), b3=*LDSP(u32x2,ap+24); const u32x4 a0=cat(b0,b1), a1=cat(b2,b3); const unsigned a2=*LDSP(unsigned,ap+32);
    #pragma unroll
    for(int t=0;t<2;t++){ f32x16 z;
      asm volatile("s_waitcnt vmcnt(%3)" : "+v"(q0[t]), "+v"(q1[t]), "+v"(q2[t]) : "i"((t? 2:3)+SLA));
      mz32(z,a0,q0[t]); mv32<true>(z,a1,q1[t]); mv32<true>(z,u32x4{a2,0,0,0},u32x4{q2[t],0,0,0}); asm volatile("s_nop 7\n\ts_nop 3" : "+v"(z));
      const float rc=__builtin_amdgcn_rcpf(z[10]+1e-6f);
      const unsigned so=WS_OF+(tr*8+w*2+t)*36*1024;
      bstore128_sc1(u32x4{pk2(z[0]*rc,z[1]*rc),pk2(z[2]*rc,z[3]*rc),pk2(z[4]*rc,z[5]*rc),pk2(z[6]*rc,z[7]*rc)}, vl, srdws, so+head*1024);
      bstore32_sc1(pk2(z[8]*rc,z[9]*rc), h? 0x80000000u : vl, srdws, so+(32+(head>>3))*1024+((head>>2)&1)*512+(head&3)*4); }
    STAMPIT(17);
    { const u32x4 srdx=make_srd(x+(size_t)(tb*64+w*16)*D_+cg*72, 16*D_*2); const unsigned vx=l15*1152+g*8;
      bload64<0,false>(xr[0],vx,srdx,0); bload64<32,false>(xr[1],vx,srdx,0); bload64<64,false>(xr[2],vx,srdx,0); bload64<96,false>(xr[3],vx,srdx,0); bload64<128,false>(xr[4],g<2?vx:0x80000000u,srdx,0);
    }
    SLAB(SLA,SLB)
    WAITV(5+SLB-SLA);
    flag_store(flags+(tr*4+w)*128+64+head, vtag);
    STAMPIT(2);
    SLAB(SLB,24)
    #undef SLAB
  }
  WST(0,2)
  poll_all(flags+tb*128+64, (lane&31)*4, w, tag);
  if(w==3) WAITV(0); else WAITV(24);
  BAR();
  if(blockIdx.x==0 && w==3) flag_store(flags+64*128+64, vtag);
  bload32<0,true>(mx,l15*4,srdws,WS_MEANX+(tb*64+w*16)*4);
  STAMPIT(3);
  {
    u32x4 o[18];
    { const unsigned vo=(g>>1)*1024+((g&1)*32+(w&1)*16+l15)*16, so=WS_OF+(tb*2+(w>>1))*36*1024;
      #define LD_(k) bload128<((k)&1)*2048,true>(o[k], vo, srdws, so+((k)>>1)*4096);
      LD_(0) LD_(1) LD_(2) LD_(3) LD_(4) LD_(5) LD_(6) LD_(7) LD_(8) LD_(9) LD_(10) LD_(11) LD_(12) LD_(13) LD_(14) LD_(15) LD_(16) LD_(17)
      #undef LD_
    }
    f32x4 acc[5];
    const unsigned wb=ldsb+FR_SLAB+(4*g+q)*160+p*8;
    u32x4 a[3][5];
    #define K2_RD(S) _Pragma("unroll") for(int j=0;j<5;j++) a[(S)%3][j]=trfrag(wb+(S)*5120+j*32, wb+(S)*5120+2560+j*32);
    K2_RD(0) K2_RD(1)
    #pragma unroll
    for(int S=0;S<18;S++){
      if(S+2<18) { K2_RD(S+2) }
      SB();
      asm volatile("s_waitcnt vmcnt(%1)" : "+v"(o[S]) : "i"(17-S));
      #pragma unroll
      for(int j=0;j<5;j++){ if(S==0) asm volatile("v_mfma_f32_16x16x32_bf16 %0, %1, %2, 0" : "=&v"(acc[j]) : "v"(a[0][j]), "v"(o[0])); else mv16(acc[j],a[S%3][j],o[S]); }
      SB(); }
    #undef K2_RD
    asm volatile("s_nop 7" : "+v"(acc[0]), "+v"(acc[1]), "+v"(acc[2]), "+v"(acc[3]), "+v"(acc[4]));
    BAR();
    STAMPIT(4);
    asm volatile("" : "+v"(xr[0]), "+v"(xr[1]), "+v"(xr[2]), "+v"(xr[3]), "+v"(xr[4]), "+v"(mx));
    const float m=__uint_as_float(mx)+row_bcast2(acc[4][0]+acc[4][1])*(1.f/D_);
    float s2=0.f;
    #pragma unroll
    for(int j=0;j<5;j++){ const float v0=acc[j][0]+(lo2f(xr[j][0])-m), v1=acc[j][1]+(hi2f(xr[j][0])-m), v2=acc[j][2]+(lo2f(xr[j][1])-m), v3=acc[j][3]+(hi2f(xr[j][1])-m);
      const int col=cg*72+16*j+4*g; const bool ok= j<4 || g<2;
      bstore64_sc1(u32x2{pk2(v0,v1),pk2(v2,v3)}, ok? (unsigned)((col>>5)*1024+(l15+16*((col>>2)&3))*16+((col>>4)&1)*8) : 0x80000000u, srdws, WS_HF+(tb*4+w)*18*1024);
      s2+= ok? v0*v0+v1*v1+v2*v2+v3*v3 : 0.f; }
    s2=rows_sum(s2);
    STAMPIT(19);
    bstore32_sc1(__float_as_uint(g? m:s2), g==0? (unsigned)(WS_SQ+((tb*8+cg)*64+l15*4+w)*4) : (g==1 && cg==0)? (unsigned)(WS_MB+(tb*64+w*16+l15)*4) : 0x80000000u, srdws, 0);
    WAITV(0);
    flag_store(flags+tb*128+96+cg*4+w, vtag);
    STAMPIT(5);
  }
  WST(2,6)
  #undef WST
  c.srdact=make_srd(ws+WS_ACT, S_*F_*2);
  const unsigned f2=2*(g&1)+(q>>1);
  const unsigned bfull=ldsb+(4*g+q)*128+(((2*fh)^f2)*32)+p*8, bhalf=ldsb+(4*g+q)*128+(((2*isB+fh)^f2)*32)+p*8;
  #pragma unroll
  for(int k=0;k<3;k++){ c.bA[k]=bfull+k*2*P1_SLOT; c.bB[k]=(bfull^32)+k*2*P1_SLOT; }
  c.stoff=lane*16+th*4096+fh*8192;
  const int fc=w&1, ch=isB;
  d.srdw=make_srd((const char*)wd+(size_t)xcd*576*1152-4096, 576*1152+4096);
  d.srda=make_srd(ws+WS_ACT+(size_t)(T*144+xcd*18)*8192-4096, 18*8192+4096);
  d.m0w=ldsb+w*P2_WREG; d.sowb=(unsigned)(8*w*1152); d.soab=(unsigned)(w*2048);
  auto kst=[&](int s){ const int pr=s>>1; const int who = pr<4 ? (pr&1) : (pr>=6); const int j = pr<4 ? (pr>>1) : 2+(pr&1);
    return s<16 ? 2*(((who^isB)?5:0)+((j+T)&3))+(s&1) : 8+((s&1)^isB); };
  u64 tabA=0, tabB=0;
  #pragma unroll
  for(int s=1;s<6;s++) tabA|=(u64)kst(s)<<(5*(s-1));
  #pragma unroll
  for(int s=6;s<18;s++) tabB|=(u64)kst(s)<<(5*(s-6));
  asm volatile("" : "+s"(tabA), "+s"(tabB));
  d.sow=d.sowb+kst(0)*36864u; d.soa=d.soab+kst(0)*8192u; d.tab=tabA;
  d.myflag=flags+me*128; d.pflag=flags+(me^8)*128; d.tag=tag; d.vtag=vtag;
  poll_all(flags+T*256+96, (lane&31)*4+(lane>>5)*512, w, tag);
  BAR();
  STAMPIT(6);
  { u32x4 sv[2]; c.vl=lane*16; c.srdx=make_srd(ws+WS_HF+(size_t)(T*8+th*4)*18*1024, 4*18*1024);
    c.vwarm=(4096+((T*2+isB)*18+w*6)*1152+lane*128)|((lane<54 && w<3)? 0:0x80000000u);
    bload128<0,true>(sv[0], l15*16+g*512, srdws, WS_SQ+(T*2+th)*2048); bload128<256,true>(sv[1], l15*16+g*512, srdws, WS_SQ+(T*2+th)*2048);
    #define XL(k) p1_xload<k,0>(c); p1_xload<k,1>(c); p1_xload<k,2>(c); p1_xload<k,3>(c);
    XL(0) XL(1) XL(2) XL(3) XL(4) XL(5)
    #undef XL
    WAITV(12);
    asm volatile("" : "+v"(sv[0]), "+v"(sv[1]));
    #pragma unroll
    for(int t=0;t<4;t++){ const float v=rows_sum(__uint_as_float(sv[0][t])+__uint_as_float(sv[1][t]))*(1.f/D_)+1e-5f;
      c.ir2[t]=v; c.c1[t]=-1.4426950408889634f*__builtin_amdgcn_rsqf(v); asm volatile("" : "+v"(c.c1[t]), "+v"(c.ir2[t])); } }
  BAR();
  STAMPIT(7);
  #pragma unroll
  for(int k=0;k<2;k++){ trread<0>(c.flo[k][0],c.bA[0]+k*4096); trread<2048>(c.fhi[k][0],c.bA[0]+k*4096); trread<P1_REG>(c.flo[k][1],c.bA[0]+k*4096); trread<P1_REG+2048>(c.fhi[k][1],c.bA[0]+k*4096); }
  unsigned utab=unit_of(0)|unit_of(1)<<4|unit_of(2)<<8|unit_of(3)<<12|4<<16; const unsigned sob=xcd*9*128+rowoff, stb=(T*144+xcd*18)*8192;
  asm volatile("" : "+s"(utab));
  c.so_next=sob+((utab>>4)&15)*128;
  p1_unit<0>(c, d, 0);
  c.st_prev=stb+(utab&15)*16384; c.so_cur=c.so_next; utab>>=4;
  STAMPIT(15);
  #pragma unroll 1
  for(int it=1;it<4;it++){
    const unsigned un=utab&15; utab>>=4;
    c.so_next=sob+(utab&15)*128;
    p1_unit<1>(c, d, bfull^(it==3? bfull^bhalf : 0));
    c.st_prev=stb+un*16384; c.so_cur=c.so_next;
  }
  asm volatile("" :: "v"(c.vwarm));
  STAMPIT(8);
  flag_store(d.myflag,d.vtag);
  int ln2=threadIdx.x&63; asm volatile("" : "+v"(ln2));
  { const int cc=ln2&7; const unsigned v0=(ln2>>3)*1152+(((cc>>1)^((ln2>>4)&3))*32)+(cc&1)*16+ch*640+4096;
    #pragma unroll
    for(int j=0;j<4;j++) d.vw[j]=v0-j*896;
    const int c4=ln2&3, r=(ln2>>2)&7; d.vw[4]= ln2<32? r*1152+512+ch*64+(((c4>>1)^((r>>2)&1))*32)+(c4&1)*16+4096 : 0x80000000u;
    d.va=ln2*16+4096-512; }
  p1_unit<2>(c, d, 0);
  c.st_prev=(unsigned)((T*144+xcd*18+8+isB)*8192);
  STAMPIT(9);
  #pragma unroll
  for(int j=0;j<7;j++) p2_issue<4>(d,j);
  p2_next(d); d.tab=tabB;
  { asm volatile("" : "+v"(ln2)); const int lane=ln2, q=(lane&15)>>2, p=lane&3; const int g=lane>>4; const int f=2*(g&1)+(q>>1);
    c.stoff=lane*16+th*4096+fh*8;
    #pragma unroll
    for(int sp=0;sp<3;sp++){
      #pragma unroll
      for(int jj=0;jj<4;jj++) d.ba[sp][jj]=ldsb+sp*2*P2_SLOT+(g>>1)*P2_WREG+fc*2048+(4*(g&1)+q)*128+8*p+((jj^f)*32);
      d.ba[sp][4]=ldsb+sp*2*P2_SLOT+(g>>1)*P2_WREG+4096+(4*(g&1)+q)*64+8*p+((fc^(g&1))*32);
      d.bb[sp]=ldsb+sp*2*P2_SLOT+2*th*P2_WREG+4608+lane*16; } }
  ldsread128a<0>(d.bf[0][0],d.bb[0]); ldsread128a<1024>(d.bf[0][1],d.bb[0]); ldsread128a<P2_WREG>(d.bf[0][2],d.bb[0]); ldsread128a<P2_WREG+1024>(d.bf[0][3],d.bb[0]);
  trread<0>(d.flo[0],d.ba[0][0]); trread<2*P2_WREG>(d.fhi[0],d.ba[0][0]); trread<0>(d.flo[1],d.ba[0][1]); trread<2*P2_WREG>(d.fhi[1],d.ba[0][1]); trread<0>(d.flo[2],d.ba[0][2]); trread<2*P2_WREG>(d.fhi[2],d.ba[0][2]);
  p2_stage<0,true>(d,c);
  #pragma unroll 1
  for(int s=0;s<2;s++){ p2_stage<1,false>(d,c); p2_stage<2,false>(d,c); p2_stage<3,false>(d,c); p2_stage<4,false>(d,c); p2_stage<5,false>(d,c); p2_stage<0,false>(d,c); }
  STAMPIT(10);
  asm volatile("" : "+v"(ln2));
  { const int lane=ln2, l15=lane&15, g=lane>>4;
  d.srdp=make_srd(ws+WS_PART, 8*S_*D_*2); d.vl=lane*16; d.ownl=ldsb+lane*16;
  #pragma unroll
  for(int u=0;u<2;u++){     unsigned o; asm volatile("s_cmp_eq_u32 %2, %3\n\ts_cselect_b32 %0, 0x80000000, %4\n\ts_cselect_b32 %1, 0, 9216" : "=&s"(d.pb[u]), "=&s"(o) : "s"(2*w+u), "s"(xcd), "s"((unsigned)(((((T*2+ch)*8+2*w+u)*8+xcd)*9)*1024)) : "scc");
    d.ownw[u]=d.ownl+o; }
  p2_tgroup<0>(d); p2_tgroup<1>(d);
  const int ow=xcd>>1, ou=xcd&1; const int t16=T*8+(ow>>1)*4+2*ou;
  u32x4 hr[2]; unsigned h8, mA, mB, m8;
  const int ks=ch*10+(ow&1)*4+w;
  bload128<0,true>(hr[0], d.vl, srdws, WS_HF+(t16*18+ks)*1024); bload128<0,true>(hr[1], d.vl, srdws, WS_HF+((t16+1)*18+ks)*1024);
  bload32<0,true>(h8, ((lane>>1)&1)*18*1024+(16*w+(lane>>2))*16+(lane&1)*4, srdws, WS_HF+(t16*18+8+ch)*1024+(ow&1)*8);
  bload32<0,true>(mA, l15*4, srdws, WS_MB+t16*64); bload32<64,true>(mB, l15*4, srdws, WS_MB+t16*64); bload32<0,true>(m8, (((lane>>1)&1)*16+(lane>>2))*4, srdws, WS_MB+t16*64);
  p2_tgroup<2>(d);
  STAMPIT(11);
  asm volatile("s_nop 7" ::: "memory");
  p2_dump_all<0>(d);
  asm volatile("s_waitcnt vmcnt(0) lgkmcnt(0)" ::: "memory");
  asm volatile("s_mov_b64 exec, 3\n\tglobal_store_dword %0, %1, %2 sc1\n\ts_mov_b64 exec, -1" :: "v"(lane*512), "v"(d.vtag), "s"(flags+(((me&~7)+2*w)*128+32+xcd)) : "memory");
  STAMPIT(12);
  poll_all(flags+me*128+32, (lane&7)*4, w, tag);
  BAR();
  STAMPIT(13);
  u32x4 v[8][2]; unsigned v8[8];
  { const unsigned sb=(unsigned)(((T*2+ch)*8+xcd)*8*9*1024);
    #pragma unroll
    for(int s=1;s<8;s++){ const unsigned so=sb+((xcd+s)&7)*9*1024; bload128<0,true>(v[s][0], d.vl, d.srdp, so+w*2048); bload128<1024,true>(v[s][1], d.vl, d.srdp, so+w*2048); bload32<0,true>(v8[s], lane*4, d.srdp, so+8192+w*256); }
    v[0][0]=*LDSP(u32x4,d.ownl+w*2048); v[0][1]=*LDSP(u32x4,d.ownl+w*2048+1024); v8[0]=*LDSP(unsigned,ldsb+8192+w*256+lane*4); }
  asm volatile("" : "+v"(hr[0]), "+v"(hr[1]), "+v"(h8), "+v"(mA), "+v"(mB), "+v"(m8));
  STAMPIT(20);
  float s[2][8], s8[2];
  #pragma unroll
  for(int k=0;k<2;k++){ const float a=__uint_as_float(mA), b=__uint_as_float(mB);
    s[k][0]=lo2f(hr[0][2*k])+a; s[k][1]=hi2f(hr[0][2*k])+a; s[k][2]=lo2f(hr[0][2*k+1])+a; s[k][3]=hi2f(hr[0][2*k+1])+a;
    s[k][4]=lo2f(hr[1][2*k])+b; s[k][5]=hi2f(hr[1][2*k])+b; s[k][6]=lo2f(hr[1][2*k+1])+b; s[k][7]=hi2f(hr[1][2*k+1])+b; }
  s8[0]=lo2f(h8)+__uint_as_float(m8); s8[1]=hi2f(h8)+__uint_as_float(m8);
  #pragma unroll
  for(int x_=0;x_<8;x_++){
    if(x_) asm volatile("s_waitcnt vmcnt(%3)" : "+v"(v[x_][0]), "+v"(v[x_][1]), "+v"(v8[x_]) : "i"(3*(7-x_)));
    #pragma unroll
    for(int k=0;k<2;k++)
      #pragma unroll
      for(int e=0;e<4;e++){ s[k][2*e]=dot2(v[x_][k][e],selL,s[k][2*e]); s[k][2*e+1]=dot2(v[x_][k][e],selH,s[k][2*e+1]); }
    s8[0]=dot2(v8[x_],selL,s8[0]); s8[1]=dot2(v8[x_],selH,s8[1]); }
  STAMPIT(21);
  { u16* o=out+(size_t)(t16*16+l15)*D_+ks*32+4*g;
    *(u32x2*)o=u32x2{pk2(s[0][0],s[0][1]),pk2(s[0][2],s[0][3])}; *(u32x2*)(o+16)=u32x2{pk2(s[1][0],s[1][1]),pk2(s[1][2],s[1][3])};
    *(u32x2*)(o+16*D_)=u32x2{pk2(s[0][4],s[0][5]),pk2(s[0][6],s[0][7])}; *(u32x2*)(o+16*D_+16)=u32x2{pk2(s[1][4],s[1][5]),pk2(s[1][6],s[1][7])};
    *(unsigned*)(out+(size_t)(t16*16+((lane>>1)&1)*16+(lane>>2))*D_+256+32*ch+16*(ow&1)+4*w+2*(lane&1))=pk2(s8[0],s8[1]); }
  }
  STAMPIT(14); STAMP_END(15);
}
extern "C" void kernel_launch(void* const* d_in, const int* in_sizes, int n_in, void* d_out, int out_size, void* d_ws, size_t ws_size, hipStream_t stream){
  const u16* x=(const u16*)d_in[0]; const u16* wqkv=(const u16*)d_in[1]; const u16* wphi=(const u16*)d_in[2]; const u16* wo=(const u16*)d_in[3];
  const u16* wg=(const u16*)d_in[5]; const u16* wu=(const u16*)d_in[6]; const u16* wd=(const u16*)d_in[7];
  char* ws=(char*)d_ws;
  (void)hipFuncSetAttribute((const void*)k3, hipFuncAttributeMaxDynamicSharedMemorySize, K_LDS);
  k3<<<256,256,K_LDS,stream>>>(x,wqkv,wphi,wo,wg,wu,wd,ws,(u16*)d_out STAMP_PASS(0));
}
```

```cpp
#include <hip/hip_runtime.h>
#include <stdint.h>
#include <utility>
typedef unsigned short u16;
typedef unsigned long long u64;
using u32x2=__attribute__((ext_vector_type(2)))unsigned;
using u32x4=__attribute__((ext_vector_type(4)))unsigned;
using f32x4=__attribute__((ext_vector_type(4)))float;
using f32x16=__attribute__((ext_vector_type(16)))float;
#define DI __device__ __forceinline__
constexpr int S_=2048, D_=576, H_=32, DH_=18, F_=4608;
DI float bf2f(u16 v){ return __uint_as_float(((unsigned)v)<<16); }
DI u16 f2bf(float f){ unsigned u=__float_as_uint(f); u += 0x7fff + ((u>>16)&1); return (u16)(u>>16); }
template<unsigned M,int SH> DI unsigned divm(unsigned x){ return __umul24(x,M)>>SH; }
DI unsigned mad24(unsigned a,unsigned b,unsigned c){ return __umul24(a,b)+c; }
DI unsigned rfl(unsigned x){ return __builtin_amdgcn_readfirstlane(x); }
DI u32x4 make_srd(const void* p, unsigned bytes){ u64 a=(u64)p; u32x4 s; s[0]=rfl((unsigned)a); s[1]=rfl((unsigned)(a>>32)&0xffffu); s[2]=bytes; s[3]=0x20000u; return s; }
#define WAITL(n) __builtin_amdgcn_s_waitcnt(0xC07F|((n)<<8))
#define WAITV(n) __builtin_amdgcn_s_waitcnt(0x0F70|((n)&15)|(((n)>>4)<<14))
#define BAR() asm volatile("s_barrier" ::: "memory")
template<int OFF> DI void trread(u32x2& d, unsigned addr){ asm volatile("ds_read_b64_tr_b16 %0, %1 offset:%2" : "=v"(d) : "v"(addr), "i"(OFF)); }
template<int OFF> DI void ldsread128(u32x4& d, unsigned addr){ asm volatile("ds_read_b128 %0, %1 offset:%2" : "=v"(d) : "v"(addr), "i"(OFF)); }
template<int OFF> DI void ldsread128f(f32x4& d, unsigned addr){ asm volatile("ds_read_b128 %0, %1 offset:%2" : "=v"(d) : "v"(addr), "i"(OFF)); }
DI u32x4 cat(const u32x2& a,const u32x2& b){ return __builtin_shufflevector(a,b,0,1,2,3); }
DI void glds_m0(unsigned m0, unsigned voff, u32x4 srd, unsigned soff){
  asm volatile("s_mov_b32 m0, %0\n\ts_nop 0\n\tbuffer_load_dwordx4 %1, %2, %3 offen lds" :: "s"(m0), "v"(voff), "s"(srd), "s"(soff) : "memory");
}
template<int OFF> DI void glds_off(unsigned voff, u32x4 srd, unsigned soff){
  asm volatile("buffer_load_dwordx4 %0, %1, %2 offen offset:%3 lds" :: "v"(voff), "s"(srd), "s"(soff), "i"(OFF) : "memory");
}
DI void bstore128(u32x4 v, unsigned voff, u32x4 srd, unsigned soff){
  asm volatile("buffer_store_dwordx4 %0, %1, %2, %3 offen\n\ts_nop 1" :: "v"(v), "v"(voff), "s"(srd), "s"(soff) : "memory");
}
DI void vfma(float& d, float a, float b, float c){ asm volatile("v_fma_f32 %0, %1, %2, %3" : "=v"(d) : "v"(a), "v"(b), "v"(c)); }
DI void vmul(float& d, float a, float b){ asm volatile("v_mul_f32 %0, %1, %2" : "=v"(d) : "v"(a), "v"(b)); }
DI void vexp(float& d){ asm volatile("v_exp_f32 %0, %0" : "+v"(d)); }
DI void vrcp(float& d){ asm volatile("v_rcp_f32 %0, %0" : "+v"(d)); }
DI void vcvtpk(unsigned& d, float a, float b){ asm volatile("v_cvt_pk_bf16_f32 %0, %1, %2" : "=v"(d) : "v"(a), "v"(b)); }

#define STAMP_ARG
#define STAMP_PASS(k)
#define STAMP_DECL
#define STAMPIT(i)
#define STAMPV(i)
#define STAMP_END(n)
constexpr unsigned WS_FQ=0, WS_KVP=WS_FQ+64*32*2304, WS_OF=WS_KVP+32*32*1536, WS_HF=WS_OF+S_*D_*2, WS_SQ=WS_HF+S_*D_*2, WS_MB=WS_SQ+S_*8*4, WS_MEANX=WS_MB+S_*4,
  WS_WOROW=WS_MEANX+S_*4, WS_SMALL=WS_WOROW+8192, WS_FLAGS=WS_SMALL, WS_ACT=WS_FLAGS+256*512, WS_PART=WS_ACT+S_*F_*2, WS_END=WS_PART+8*S_*D_*2;
using s16x4=__attribute__((ext_vector_type(4)))short;
using bf16x8=__attribute__((ext_vector_type(8)))__bf16;
using f32x2=__attribute__((ext_vector_type(2)))float;
using bf16x2=__attribute__((ext_vector_type(2)))__bf16;
#define LDSP(T,a) ((__attribute__((address_space(3))) T*)(a))
DI u32x2 trb(unsigned a){ return __builtin_bit_cast(u32x2, __builtin_amdgcn_ds_read_tr16_b64_v4i16(LDSP(s16x4,a))); }
DI u32x4 trfrag(unsigned a0, unsigned a1){ return cat(trb(a0),trb(a1)); }
DI f32x16 mfma32(u32x4 a,u32x4 b,f32x16 c){ return __builtin_amdgcn_mfma_f32_32x32x16_bf16(__builtin_bit_cast(bf16x8,a),__builtin_bit_cast(bf16x8,b),c,0,0,0); }
DI f32x4 mfma16(u32x4 a,u32x4 b,f32x4 c){ return __builtin_amdgcn_mfma_f32_16x16x32_bf16(__builtin_bit_cast(bf16x8,a),__builtin_bit_cast(bf16x8,b),c,0,0,0); }
DI unsigned pk2(float lo,float hi){ f32x2 v={lo,hi}; return __builtin_bit_cast(unsigned,__builtin_convertvector(v,bf16x2)); }
DI float lo2f(unsigned v){ return __uint_as_float(v<<16); }
DI float hi2f(unsigned v){ return __uint_as_float(v&0xffff0000u); }
DI float xhalf_max(float v){ auto r=__builtin_amdgcn_permlane32_swap(__float_as_uint(v),__float_as_uint(v),false,false); return fmaxf(__uint_as_float(r[0]),__uint_as_float(r[1])); }
DI float xhalf_min(float v){ auto r=__builtin_amdgcn_permlane32_swap(__float_as_uint(v),__float_as_uint(v),false,false); return fminf(__uint_as_float(r[0]),__uint_as_float(r[1])); }
DI float xhalf_sum(float v){ auto r=__builtin_amdgcn_permlane32_swap(__float_as_uint(v),__float_as_uint(v),false,false); return __uint_as_float(r[0])+__uint_as_float(r[1]); }
DI u32x4 packfrag(const f32x16& a,int s){ return u32x4{pk2(a[8*s],a[8*s+1]),pk2(a[8*s+2],a[8*s+3]),pk2(a[8*s+4],a[8*s+5]),pk2(a[8*s+6],a[8*s+7])}; }
DI void hedgehog(const f32x16& p, float r, unsigned (&o)[9]){
  float mx=fmaxf(fmaxf(p[0],p[1]),p[2]), mn=fminf(fminf(p[0],p[1]),p[2]);
  #pragma unroll
  for(int i=3;i<9;i+=2){ mx=fmaxf(fmaxf(mx,p[i]),p[i+1]); mn=fminf(fminf(mn,p[i]),p[i+1]); }
  mx=xhalf_max(mx); mn=xhalf_min(mn);
  const float L=1.4426950408889634f*r; const f32x2 LL={L,-L}, ab={-mx*L,mn*L};
  f32x2 e[9], s={0.f,0.f};
  #pragma unroll
  for(int i=0;i<9;i++){ const f32x2 t=__builtin_elementwise_fma(f32x2{p[i],p[i]},LL,ab); e[i]=f32x2{__builtin_amdgcn_exp2f(t[0]),__builtin_amdgcn_exp2f(t[1])}; s= i? s+e[i] : e[i]; }
  s=f32x2{__builtin_amdgcn_rcpf(xhalf_sum(s[0])),__builtin_amdgcn_rcpf(xhalf_sum(s[1]))};
  #pragma unroll
  for(int i=0;i<9;i++){ const f32x2 t=e[i]*s; o[i]=pk2(t[0],t[1]); }
}
template<bool P> DI void mv32(f32x16& d, const u32x4& a, const u32x4& b){
  if constexpr (P) asm volatile("s_nop 1\n\tv_mfma_f32_32x32x16_bf16 %0, %1, %2, %0" : "+v"(d) : "v"(a), "v"(b)); else asm volatile("v_mfma_f32_32x32x16_bf16 %0, %1, %2, %0" : "+v"(d) : "v"(a), "v"(b)); }
DI void mz32(f32x16& d, const u32x4& a, const u32x4& b){ asm volatile("s_nop 1\n\tv_mfma_f32_32x32x16_bf16 %0, %1, %2, 0" : "=&v"(d) : "v"(a), "v"(b)); }
#define MPAD(a,b) asm volatile("s_nop 7\n\ts_nop 3" : "+v"(a), "+v"(b))
template<int CTRL> DI float dpp_add(float v){ return v+__builtin_bit_cast(float,__builtin_amdgcn_update_dpp(0,__builtin_bit_cast(int,v),CTRL,0xf,0xf,true)); }
DI float dot2(unsigned a,unsigned b,float c){ return __builtin_amdgcn_fdot2_f32_bf16(__builtin_bit_cast(bf16x2,a),__builtin_bit_cast(bf16x2,b),c,false); }
constexpr int K1_RING=73728, K1_SLOT=13824, K1_PAIR=28672, K1_WPHI=K1_RING+2*K1_PAIR+K1_SLOT+2048, K1_STAT=49152, K_LDS=K1_RING+3*K1_PAIR;
constexpr int k1_slot(int s){ return (s>>1)*K1_PAIR+(s&1)*K1_SLOT; }
DI void mv16(f32x4& d, const u32x4& a, const u32x4& b){ asm volatile("v_mfma_f32_16x16x32_bf16 %0, %1, %2, %0" : "+v"(d) : "v"(a), "v"(b)); }
#define SB() __builtin_amdgcn_sched_barrier(0)
constexpr int P1_SLOT=24576, P1_REG=12288;
constexpr int P2_WREG=6656, P2_SLOT=4*P2_WREG;
constexpr int FR_SLAB=53248, FR_KVB=FR_SLAB+576*160;
#define MAP 1
#define CODE_BYTES 46000
#define WQW 60
#define NOFW 0
#define SLA 4
#define SLB 12
#define OUTST(T,p,v) *(T*)(p)=v
#define GAS __attribute__((address_space(1)))
struct P1 {
  u32x4 x[18][4];
  f32x4 acc[2][2][4];
  float tz[2][4][4], tp[2][4][4];
  u32x2 flo[3][2], fhi[3][2];
  unsigned pk[2][4];
  float c1[4], ir2[4];
  unsigned bA[3], bB[3];
  unsigned voff, stoff, vl, vwarm;
  u32x4 srdw, srdact, srdx;
  unsigned m0base, so_cur, so_next, st_prev;
};
struct P2 {
  f32x4 acc[9][4];
  u32x2 flo[6], fhi[6];
  u32x4 bf[2][4], bt[4][4];
  unsigned ba[3][5], bb[3];
  unsigned vw[5], va;
  u32x4 srdw, srda, srdp;
  unsigned m0w, sowb, soab, sow, soa, kk;
  u64 tab;
  GAS unsigned* myflag; GAS unsigned* pflag; GAS unsigned* f1; GAS unsigned* f2; unsigned pv, tag, vtag;
  unsigned r[4], vl, ownl, pb[2], ownw[2];
};
DI void bstore128_sc1(u32x4 v, unsigned voff, u32x4 srd, unsigned soff){
  asm volatile("buffer_store_dwordx4 %0, %1, %2, %3 offen sc1\n\ts_nop 1" :: "v"(v), "v"(voff), "s"(srd), "s"(soff) : "memory");
}
DI void bstore64_sc1(u32x2 v, unsigned voff, u32x4 srd, unsigned soff){
  asm volatile("buffer_store_dwordx2 %0, %1, %2, %3 offen sc1" :: "v"(v), "v"(voff), "s"(srd), "s"(soff) : "memory");
}
DI void bstore32_sc1(unsigned v, unsigned voff, u32x4 srd, unsigned soff){
  asm volatile("buffer_store_dword %0, %1, %2, %3 offen sc1" :: "v"(v), "v"(voff), "s"(srd), "s"(soff) : "memory");
}
template<int OFF> DI void glds_off_sc1(unsigned voff, u32x4 srd, unsigned soff){
  asm volatile("buffer_load_dwordx4 %0, %1, %2 offen offset:%3 sc1 lds" :: "v"(voff), "s"(srd), "s"(soff), "i"(OFF) : "memory");
}
template<int OFF> DI void set_m0(unsigned base){ asm volatile("s_add_u32 m0, %0, %1" :: "s"(base), "i"(OFF) : "scc", "memory"); }
template<int OFF> DI void glds_half(unsigned voff, u32x4 srd, unsigned soff){
  asm volatile("buffer_load_dwordx4 %0, %1, %2 offen offset:%3 lds" :: "v"(voff), "s"(srd), "s"(soff), "i"(OFF) : "memory");
}
template<int OFF> DI void ldsread128a(u32x4& d, unsigned addr){ asm volatile("ds_read_b128 %0, %1 offset:%2" : "=a"(d) : "v"(addr), "i"(OFF)); }
template<int OFF,bool SC1> DI void bload128(u32x4& d, unsigned voff, u32x4 srd, unsigned soff){
  if constexpr (SC1) asm volatile("buffer_load_dwordx4 %0, %1, %2, %3 offen offset:%4 sc1" : "=v"(d) : "v"(voff), "s"(srd), "s"(soff), "i"(OFF) : "memory");
  else asm volatile("buffer_load_dwordx4 %0, %1, %2, %3 offen offset:%4" : "=v"(d) : "v"(voff), "s"(srd), "s"(soff), "i"(OFF) : "memory");
}
template<int OFF,bool SC1> DI void bload64(u32x2& d, unsigned voff, u32x4 srd, unsigned soff){
  if constexpr (SC1) asm volatile("buffer_load_dwordx2 %0, %1, %2, %3 offen offset:%4 sc1" : "=v"(d) : "v"(voff), "s"(srd), "s"(soff), "i"(OFF) : "memory");
  else asm volatile("buffer_load_dwordx2 %0, %1, %2, %3 offen offset:%4" : "=v"(d) : "v"(voff), "s"(srd), "s"(soff), "i"(OFF) : "memory");
}
template<int OFF,bool SC1> DI void bload32(unsigned& d, unsigned voff, u32x4 srd, unsigned soff){
  if constexpr (SC1) asm volatile("buffer_load_dword %0, %1, %2, %3 offen offset:%4 sc1" : "=v"(d) : "v"(voff), "s"(srd), "s"(soff), "i"(OFF) : "memory");
  else asm volatile("buffer_load_dword %0, %1, %2, %3 offen offset:%4" : "=v"(d) : "v"(voff), "s"(srd), "s"(soff), "i"(OFF) : "memory");
}
DI void flag_store(GAS unsigned* p, unsigned v){
  asm volatile("global_store_dword %0, %1, %2 sc1" :: "v"(0u), "v"(v), "s"(p) : "memory");
}
DI void flag_load(unsigned& d, GAS unsigned* p){ asm volatile("global_load_dword %0, %1, %2 sc1" : "=v"(d) : "v"(0u), "s"(p) : "memory"); }
DI void flag_check(unsigned& v, GAS unsigned* p, unsigned need){
  unsigned s_, n_;
  asm volatile("s_nop 0\n\tv_readfirstlane_b32 %0, %2\n\ts_cmp_eq_u32 %0, %5\n\ts_cbranch_scc1 .Ldone%=\n\ts_mov_b32 %1, 0x100000\n"
               ".Lspin%=:\n\ts_sleep 2\n\tglobal_load_dword %2, %3, %4 sc1\n\ts_waitcnt vmcnt(0)\n\tv_readfirstlane_b32 %0, %2\n\ts_cmp_eq_u32 %0, %5\n\ts_cbranch_scc1 .Ldone%=\n\t"
               "s_sub_u32 %1, %1, 1\n\ts_cbranch_scc0 .Lspin%=\n.Ldone%=:"
               : "=&s"(s_), "=&s"(n_), "+&v"(v) : "v"(0u), "s"(p), "s"(need) : "scc", "memory");
}
DI void poll_all(GAS unsigned* p, unsigned voff, unsigned skip, unsigned tag){
  unsigned v_, n_;
  asm volatile("s_cmp_lg_u32 %4, 0\n\ts_cbranch_scc1 .Ldone%=\n\ts_mov_b32 %1, 0x100000\n"
               ".Lspin%=:\n\tglobal_load_dword %0, %2, %3 sc1\n\ts_waitcnt vmcnt(0)\n\tv_cmp_ne_u32 vcc, %5, %0\n\ts_cbranch_vccz .Ldone%=\n\t"
               "s_sleep 1\n\ts_sub_u32 %1, %1, 1\n\ts_cbranch_scc0 .Lspin%=\n.Ldone%=:"
               : "=&v"(v_), "=&s"(n_) : "v"(voff), "s"(p), "s"(skip), "s"(tag) : "scc", "vcc", "memory");
}
template<int Z,bool AG> DI void p1_mfma(f32x4& acc, const u32x4& a, const u32x4& x){
  if constexpr (Z==2)  asm volatile("v_mfma_f32_16x16x32_bf16 %0, %1, %2, 0" : "=&v"(acc) : "v"(a), "a"(x));
  if constexpr (Z==1 && AG)  asm volatile("v_mfma_f32_16x16x32_bf16 %0, %1, %2, 0" : "+v"(acc) : "v"(a), "a"(x));
  if constexpr (!Z && AG) asm volatile("v_mfma_f32_16x16x32_bf16 %0, %1, %2, %0" : "+v"(acc) : "v"(a), "a"(x));
  if constexpr (!AG)      asm volatile("v_mfma_f32_16x16x32_bf16 %0, %1, %2, %0" : "+v"(acc) : "v"(a), "v"(x));
}
template<int TL,int I> DI void p1_dr(P1& c){
  constexpr int tt=(I>>2)&3, r=I&3;
  if constexpr (I<16) vmul(c.tz[TL][tt][r], c.acc[TL][0][tt][r], c.c1[tt]); else vmul(c.tp[TL][tt][r], c.acc[TL][0][tt][r], c.acc[TL][1][tt][r]);
}
constexpr int dr_cum(int G){ int n=0; for(int g=0;g<G;g++){ const int m=g&7; n+= m==6?0 : (m&1)?1:2; if(g==1||g==9) n++; } return n; }
template<int TL,int LO,int HI> DI void p1_drs(P1& c){ if constexpr (LO<HI){ p1_dr<TL,LO>(c); p1_drs<TL,LO+1,HI>(c);} }
template<int TT,int HF,int G> DI void p1_grp(P1& c){
  float* z=c.tz[HF][TT]; float* p=c.tp[HF][TT];
  if constexpr (G==0) vexp(z[0]); if constexpr (G==2) vexp(z[1]); if constexpr (G==4) vexp(z[2]); if constexpr (G==8) vexp(z[3]);
  if constexpr (G==5) vfma(z[0],z[0],c.ir2[TT],c.ir2[TT]); if constexpr (G==7) vfma(z[1],z[1],c.ir2[TT],c.ir2[TT]);
  if constexpr (G==9) vfma(z[2],z[2],c.ir2[TT],c.ir2[TT]); if constexpr (G==13) vfma(z[3],z[3],c.ir2[TT],c.ir2[TT]);
  if constexpr (G==10) vrcp(z[0]); if constexpr (G==12) vrcp(z[1]); if constexpr (G==16) vrcp(z[2]); if constexpr (G==18) vrcp(z[3]);
  if constexpr (G==15) vmul(p[0],p[0],z[0]); if constexpr (G==17) vmul(p[1],p[1],z[1]); if constexpr (G==21) vmul(p[2],p[2],z[2]); if constexpr (G==23) vmul(p[3],p[3],z[3]);
}
template<int TT,int HF,int G> DI void p1_cvt(P1& c){ float* p=c.tp[HF][TT];
  if constexpr (G==1) vcvtpk(c.pk[TT&1][2*HF],p[0],p[1]); if constexpr (G==3) vcvtpk(c.pk[TT&1][2*HF+1],p[2],p[3]); }
template<int TT> DI void p1_store(P1& c){ const unsigned* k=c.pk[TT&1]; asm volatile("buffer_store_dwordx4 %0, %1, %2, %3 offen sc1" :: "v"(u32x4{k[0],k[1],k[2],k[3]}), "v"(c.stoff), "s"(c.srdact), "s"(c.st_prev+TT*1024) : "memory"); }
template<int SL> DI void p2_issue(P2& d, int j){
  if(j==0) { set_m0<SL*P2_SLOT>(d.m0w); asm volatile("s_nop 0"); glds_off<0>(d.vw[0],d.srdw,d.sow); }
  if(j==1) glds_off<1024>(d.vw[1],d.srdw,d.sow);
  if(j==2) glds_off<2048>(d.vw[2],d.srdw,d.sow);
  if(j==3) { glds_off<3072>(d.vw[3],d.srdw,d.sow); set_m0<SL*P2_SLOT+4096>(d.m0w); }
  if(j==4) glds_half<0>(d.vw[4],d.srdw,d.sow);
  if(j==5) glds_off_sc1<512>(d.va,d.srda,d.soa);
  if(j==6) glds_off_sc1<1536>(d.va,d.srda,d.soa);
}
DI void p2_next(P2& d){ asm volatile("s_and_b32 %0, %3, 31\n\ts_lshr_b64 %2, %2, 5\n\ts_mul_i32 %1, %0, 0x9000\n\ts_lshl_b32 %0, %0, 13\n\ts_add_u32 %1, %1, %4\n\ts_add_u32 %0, %0, %5"
   : "=&s"(d.soa), "=&s"(d.sow), "+s"(d.tab) : "s"((unsigned)d.tab), "s"(d.sowb), "s"(d.soab) : "scc"); }
template<int ST,int PC> DI void p1_glds(P1& c){ constexpr int SL=(ST+5)%6; const unsigned so=(ST==0? c.so_cur : c.so_next) + (unsigned)((96*SL+8*PC)*9216-(PC&3)*1024); glds_off<(PC&3)*1024>(c.voff, c.srdw, so); }
template<int K,int TT> DI void p1_xload(P1& c){
  if constexpr (K<16) asm volatile("buffer_load_dwordx4 %0, %1, %2, %3 offen offset:%4 sc1" : "=a"(c.x[K][TT]) : "v"(c.vl), "s"(c.srdx), "s"((TT*18+(K&~3))*1024), "i"((K&3)*1024));
  else asm volatile("buffer_load_dwordx4 %0, %1, %2, %3 offen offset:%4 sc1" : "=v"(c.x[K][TT]) : "v"(c.vl), "s"(c.srdx), "s"((TT*18+(K&~3))*1024), "i"((K&3)*1024)); }
template<int ST,int TL,int KK,int M,int MODE> DI void p1_gap(P1& c, P2& d){
  constexpr bool FULL=MODE<2;
  constexpr int G=8*KK+M, SG=2*ST+TL, K=3*ST+KK;
  { const u32x4 a=cat(c.flo[KK][M>>2],c.fhi[KK][M>>2]); p1_mfma<(K==0? (MODE==0?2:1):0),(K<16)>(c.acc[TL][M>>2][M&3], a, c.x[K][M&3]); }
  if constexpr (M&1) {
    constexpr int N=(KK+2)%3; constexpr bool same=KK==0, toB= !same && TL==0 && FULL; constexpr int S2=(same||toB)? ST : (ST+1)%6;
    constexpr int IMM=(S2&1)*P1_SLOT+(M>>2)*P1_REG+N*4096+((M>>1)&1)*2048;
    const unsigned b=((same&&TL)||toB)? c.bB[S2>>1] : c.bA[S2>>1];
    if constexpr (FULL || ST<5 || same) { if constexpr ((M>>1)&1) trread<IMM>(c.fhi[N][M>>2], b); else trread<IMM>(c.flo[N][M>>2], b); }
  }
  if constexpr (FULL) {
    constexpr int PC=3*TL+KK;
    if constexpr (MODE==1 || ST>0) {
      if constexpr (M==4 && (PC==0||PC==4)) set_m0<((ST+5)%6)*P1_SLOT+PC*1024>(c.m0base);
      if constexpr (M==6) p1_glds<ST,PC>(c); }
    if constexpr (SG==11||(SG==0&&MODE==1)) p1_drs<(SG==0),dr_cum(G),dr_cum(G+1)>(c);
    if constexpr (MODE==1) {
      if constexpr (SG>=1 && SG<=8) p1_grp<((SG-1)>>1),((SG-1)&1),G>(c);
      if constexpr (SG>=2 && SG<=9) p1_cvt<((SG-2)>>1),((SG-2)&1),G>(c);
      if constexpr (G==20 && (SG==3||SG==5||SG==7||SG==9)) p1_store<(SG-3)/2>(c);
    } else if constexpr (MODE==0) {
      if constexpr (ST<4 && (M==0||M==2)) p1_xload<3*(ST+2)+(PC>>1),2*(PC&1)+(M>>1)>(c);
      if constexpr (G==20 && SG==9) asm volatile("buffer_load_dword %0, %0, %1, 0 offen" : "+v"(c.vwarm) : "s"(d.srdw) : "memory");
    }
  } else {
    if constexpr (ST==0) { p1_drs<1,dr_cum(G),dr_cum(G+1)>(c);
      if constexpr (M==1 && KK!=1) set_m0<5*P1_SLOT+KK*2048>(c.m0base);
      if constexpr (M==2) p1_glds<0,2*KK>(c); if constexpr (M==6) p1_glds<0,2*KK+1>(c); }
    if constexpr (ST>=1 && ST<=4) { p1_grp<ST-1,0,G>(c); p1_grp<ST-1,1,(G^1)>(c); }
    if constexpr (ST>=2) { p1_cvt<ST-2,0,G>(c); p1_cvt<ST-2,1,(G^1)>(c); }
    if constexpr (ST>=2) { constexpr int j= G==6?0: G==7?1: G==14?2: G==15?3: G==20?4: G==22?5: G==23?6: -1; if constexpr (j>=0) p2_issue<ST-2>(d,j);
      if constexpr (G==21) p1_store<ST-2>(c); if constexpr (G==23) p2_next(d); }
  }
}
template<int ST,int TL,int KK,int FULL> DI void p1_step(P1& c, P2& d){
  constexpr bool LAST=FULL==2 && ST==5;
  WAITL(LAST&&KK==2? 2:6);
  p1_gap<ST,TL,KK,0,FULL>(c,d); p1_gap<ST,TL,KK,1,FULL>(c,d); p1_gap<ST,TL,KK,2,FULL>(c,d); p1_gap<ST,TL,KK,3,FULL>(c,d);
  WAITL(LAST&&KK==2? 0: LAST&&KK==1? 4:6);
  p1_gap<ST,TL,KK,4,FULL>(c,d); p1_gap<ST,TL,KK,5,FULL>(c,d); p1_gap<ST,TL,KK,6,FULL>(c,d); p1_gap<ST,TL,KK,7,FULL>(c,d);
}
template<int ST,int TL,int FULL> DI void p1_section(P1& c, P2& d){ p1_step<ST,TL,0,FULL>(c,d); p1_step<ST,TL,1,FULL>(c,d); p1_step<ST,TL,2,FULL>(c,d); }
template<int ST,int MODE> DI void p1_stage(P1& c, P2& d, unsigned bA_next){
  if constexpr (MODE==2 && ST==2) flag_load(d.pv,d.pflag);
  if constexpr (MODE==1 && ST==2) flag_store(d.f1,d.vtag);
  p1_section<ST,0,MODE>(c,d);
  if constexpr (ST==5 && MODE==1) {
    #pragma unroll
    for(int k=0;k<3;k++) c.bA[k]=bA_next+k*2*P1_SLOT; }
  if constexpr (MODE<2) p1_section<ST,1,MODE>(c,d);
  if constexpr (MODE==0) WAITV(ST==0?12: ST<4?18: ST==4?7:19);
  if constexpr (MODE==1) WAITV(ST<2?19: ST==2?20: ST<5?21:20);
  if constexpr (MODE==2) { if constexpr (ST==0) WAITV(19); if constexpr (ST==1) WAITV(12); if constexpr (ST==2) { WAITV(8); flag_check(d.pv,d.pflag,d.tag); } if constexpr (ST==5) WAITV(16); }
  BAR();
}
template<int MODE> DI void p1_unit(P1& c, P2& d, unsigned bn){ p1_stage<0,MODE>(c,d,bn); p1_stage<1,MODE>(c,d,bn); p1_stage<2,MODE>(c,d,bn); p1_stage<3,MODE>(c,d,bn); p1_stage<4,MODE>(c,d,bn); p1_stage<5,MODE>(c,d,bn); }
template<int I> DI void p1_fin(P1& c){
  if constexpr (I<108) { constexpr int tt=I/27, k=I%27, r=k&3; f32x4 &g=c.acc[0][0][tt], &u=c.acc[0][1][tt]; float t;
    if constexpr (k<4) { vmul(t,g[r],u[r]); u[r]=t; }
    else if constexpr (k<8) { vmul(t,g[r],c.c1[tt]); g[r]=t; }
    else if constexpr (k<12) { t=g[r]; vexp(t); g[r]=t; }
    else if constexpr (k<16) { vfma(t,g[r],c.ir2[tt],c.ir2[tt]); g[r]=t; }
    else if constexpr (k<20) { t=g[r]; vrcp(t); g[r]=t; }
    else if constexpr (k<24) { vmul(t,u[r],g[r]); u[r]=t; }
    else if constexpr (k<26) vcvtpk(c.pk[tt&1][k-24],u[2*(k-24)],u[2*(k-24)+1]);
    else bstore64_sc1(u32x2{c.pk[tt&1][0],c.pk[tt&1][1]}, c.stoff, c.srdact, c.st_prev+tt*1024); }
}
template<bool Z> DI void p2_mfma(f32x4& acc, const u32x4& a, const u32x4& b){
  if constexpr (Z) asm volatile("v_mfma_f32_16x16x32_bf16 %0, %1, %2, 0" : "=&v"(acc) : "v"(a), "a"(b));
  else asm volatile("v_mfma_f32_16x16x32_bf16 %0, %1, %2, %0" : "+v"(acc) : "v"(a), "a"(b)); }
template<bool E,int I> DI void p2_fin(P1& e){ if constexpr (E) { p1_fin<3*I>(e); p1_fin<3*I+1>(e); p1_fin<3*I+2>(e); } }
template<int P,int J,bool Z> DI void p2_step(P2& c, P1& e){
  constexpr int PAR=P&1, ST=PAR*9+J, CUR=ST%6, NX=(ST+3)%6; constexpr int JN=(J+3)%9; constexpr int PN=(J+3<9)?P:(P+1)%6;
  constexpr int N = J<3?4: J==3?5: J==4?6: J<7?7: J==7?6:5;
  constexpr int IMM=(PN&1)*P2_SLOT+(JN<8?(JN>>2)*1024:0); constexpr int BI=JN<8?(JN&3):4;
  constexpr int P1n=(P+1)%6, PF=(P+5)%6;
  WAITL(N);
  u32x4 a=cat(c.flo[CUR],c.fhi[CUR]);
  p2_mfma<Z>(c.acc[J][0],a,c.bf[PAR][0]);
  trread<IMM>(c.flo[NX], c.ba[PN>>1][BI]);
  p2_fin<Z,4*J>(e);
  p2_mfma<Z>(c.acc[J][1],a,c.bf[PAR][1]);
  trread<IMM+2*P2_WREG>(c.fhi[NX], c.ba[PN>>1][BI]);
  p2_fin<Z,4*J+1>(e);
  p2_mfma<Z>(c.acc[J][2],a,c.bf[PAR][2]);
  if constexpr (J>=2 && J<=5) ldsread128a<(P1n&1)*P2_SLOT+((J-2)>>1)*P2_WREG+((J-2)&1)*1024>(c.bf[PAR^1][J-2], c.bb[P1n>>1]);
  if constexpr (J==0) set_m0<PF*P2_SLOT>(c.m0w);
  if constexpr (J==7) asm volatile("s_bfe_u32 %0, %1, %2" : "=s"(c.kk) : "s"((unsigned)c.tab), "i"(5<<16) : "scc");
  if constexpr (J==8) asm volatile("s_mul_i32 %0, %1, 0x9000" : "=s"(c.sow) : "s"(c.kk));
  p2_fin<Z,4*J+2>(e);
  p2_mfma<Z>(c.acc[J][3],a,c.bf[PAR][3]);
  if constexpr (J<=3) glds_off<J*1024>(c.vw[J], c.srdw, c.sow);
  if constexpr (J==3) set_m0<PF*P2_SLOT+4096>(c.m0w);
  if constexpr (J==4) glds_half<0>(c.vw[4], c.srdw, c.sow);
  if constexpr (J==5) glds_off_sc1<512>(c.va, c.srda, c.soa);
  if constexpr (J==6) glds_off_sc1<1536>(c.va, c.srda, c.soa);
  if constexpr (J==7) { asm volatile("s_lshl_b32 %0, %1, 13\n\ts_lshr_b64 %2, %2, 5" : "=&s"(c.soa), "+s"(c.kk), "+s"(c.tab) :: "scc"); }
  if constexpr (J==8) { asm volatile("s_add_u32 %0, %0, %2\n\ts_add_u32 %1, %1, %3" : "+s"(c.sow), "+s"(c.soa) : "s"(c.sowb), "s"(c.soab) : "scc"); }
  p2_fin<Z,4*J+3>(e);
}
template<int P,bool Z> DI void p2_stage(P2& c, P1& e){
  if constexpr (P==3) flag_store(c.myflag+1,c.vtag);
  if constexpr (P==1) flag_load(c.pv,c.f2);
  p2_step<P,0,Z>(c,e); p2_step<P,1,Z>(c,e); p2_step<P,2,Z>(c,e); p2_step<P,3,Z>(c,e); p2_step<P,4,Z>(c,e); p2_step<P,5,Z>(c,e); p2_step<P,6,Z>(c,e); p2_step<P,7,Z>(c,e); p2_step<P,8,Z>(c,e);
  if constexpr (Z) WAITV(26); else if constexpr (P==2) WAITV(14); else if constexpr (P==1) { WAITV(7); flag_check(c.pv,c.f2,c.tag); } else WAITV(21);
  BAR();
}
template<int J,int U,int K> DI void p2_dump(P2& c){
  if constexpr (K<4) vcvtpk(c.r[K],c.acc[J][2*U+(K>>1)][2*(K&1)],c.acc[J][2*U+(K>>1)][2*(K&1)+1]);
  else asm volatile("buffer_store_dwordx4 %0, %1, %2, %3 offen sc1\n\tds_write_b128 %4, %0 offset:%5\n\ts_nop 0"
                    :: "v"(u32x4{c.r[0],c.r[1],c.r[2],c.r[3]}), "v"(c.vl), "s"(c.srdp), "s"(c.pb[U]+J*1024), "v"(c.ownw[U]), "i"(J*1024) : "memory");
}
template<int GI,int I> DI void p2_dumpq(P2& c){ if constexpr (GI>=0 && I>=0 && I<30) p2_dump<3*GI+I/10,(I/5)&1,I%5>(c); }
template<int GI,int S,int JJ> DI void p2_tstep(P2& c){
  constexpr int n=(GI*5+S)*3+JJ, CUR=(n+3)%6, NX=n%6, J=3*GI+JJ;
  constexpr int SN=S<4? S+1:0, JN=S<4? J : J+3, PN=SN+1;
  constexpr int IMM=(PN&1)*P2_SLOT+(JN<8?(JN>>2)*1024:0); constexpr int BI=JN<8?(JN&3):4;
  constexpr bool RD=n+3<45;
  if constexpr (GI==0) { if constexpr (JJ==0) WAITL(S==0?4:2); if constexpr (JJ==1) WAITL(S==0?6:S<4?8:4); if constexpr (JJ==2) WAITL(S<4?8:4); }
  else WAITL(n<43? 4 : n==43? 2:0);
  const u32x4 a=cat(c.flo[CUR],c.fhi[CUR]);
  const u32x4* b= S==0? c.bf[1] : c.bt[S-1<0?0:S-1];
  p2_mfma<false>(c.acc[J][0],a,b[0]);
  if constexpr (RD) trread<IMM>(c.flo[NX], c.ba[PN>>1][BI]);
  p2_mfma<false>(c.acc[J][1],a,b[1]);
  if constexpr (RD) trread<IMM+2*P2_WREG>(c.fhi[NX], c.ba[PN>>1][BI]);
  p2_mfma<false>(c.acc[J][2],a,b[2]);
  if constexpr (GI==0 && S<4 && JJ<2) ldsread128a<((S+2)&1)*P2_SLOT+JJ*P2_WREG>(c.bt[S][2*JJ], c.bb[(S+2)>>1]);
  p2_dumpq<GI-1,(S*3+JJ-1)*3>(c); p2_dumpq<GI-1,(S*3+JJ-1)*3+1>(c);
  p2_mfma<false>(c.acc[J][3],a,b[3]);
  if constexpr (GI==0 && S<4 && JJ<2) ldsread128a<((S+2)&1)*P2_SLOT+JJ*P2_WREG+1024>(c.bt[S][2*JJ+1], c.bb[(S+2)>>1]);
  p2_dumpq<GI-1,(S*3+JJ-1)*3+2>(c);
}
template<int GI,int S> DI void p2_tstage(P2& c){ p2_tstep<GI,S,0>(c); p2_tstep<GI,S,1>(c); p2_tstep<GI,S,2>(c);
  if constexpr (GI==0 && S<3) { WAITV(14-7*S); BAR(); } }
template<int GI> DI void p2_tgroup(P2& c){ p2_tstage<GI,0>(c); p2_tstage<GI,1>(c); p2_tstage<GI,2>(c); p2_tstage<GI,3>(c); p2_tstage<GI,4>(c); }
template<int I> DI void p2_dump_all(P2& c){ if constexpr (I<30){ p2_dumpq<2,I>(c); p2_dump_all<I+1>(c); } }
DI float row_bcast2(float v){
  auto a=__builtin_amdgcn_permlane32_swap(__float_as_uint(v),__float_as_uint(v),false,false);
  auto b=__builtin_amdgcn_permlane16_swap(a[1],a[1],false,false); return __uint_as_float(b[0]); }
DI float rows_sum(float v){ auto b=__builtin_amdgcn_permlane16_swap(__float_as_uint(v),__float_as_uint(v),false,false); return xhalf_sum(__uint_as_float(b[0])+__uint_as_float(b[1])); }
template<bool LO> DI unsigned slab_voff(int pc, int rr, int cc){
  const int R=6*pc+rr, S=R>>5, g=(R>>2)&3, j=4*((R>>4)&1)+(R&3), a=2*S+(g>>1), hp=g&1;
  const bool lo= LO || a<32; const unsigned row=mad24(18,lo? a : 8*(a-32)+4*hp+(j>>1),lo? 8*hp+j : 16+(j&1));
  return mad24(row,1152,cc*16); }
DI void slab_glds(unsigned m0, unsigned voff, u32x4 srd, unsigned soff){
  asm volatile("s_mov_b32 m0, %0\n\ts_mov_b64 exec, %4\n\tbuffer_load_dwordx4 %1, %2, %3 offen lds\n\ts_mov_b64 exec, -1" :: "s"(m0), "v"(voff), "s"(srd), "s"(soff), "s"(0x07FDFF7FDFF7FDFFull) : "memory"); }

__global__ __launch_bounds__(256,1) void k3(const u16* __restrict__ x, const u16* __restrict__ wqkv, const u16* __restrict__ wphi, const u16* __restrict__ wo, const u16* __restrict__ wg, const u16* __restrict__ wu, const u16* __restrict__ wd,
                                            char* __restrict__ ws, u16* __restrict__ out STAMP_ARG){
  STAMP_DECL STAMPIT(0);
  extern __shared__ __attribute__((aligned(16))) char lds[];
  asm volatile("" :: "s"(x), "s"(wqkv), "s"(wphi), "s"(wo), "s"(wg), "s"(wu), "s"(wd), "s"(ws), "s"(out));
  const int tid=threadIdx.x, lane=tid&63; const int w=rfl(tid>>6);
  const int xq=blockIdx.x&7, i=blockIdx.x>>3, isB=i&1;
  const int xcd=(xq&(8-MAP))|((i>>1)&(MAP-1)), T=(xq&(MAP-1))*(16/MAP)+(i>>1)/MAP;
  const int tb=(xq>>1)*8+(i&7), cg=(xq&1)*4+(i>>3);
  const int me=(T*2+isB)*8+xcd;
  const unsigned ldsb=(unsigned)(size_t)lds;
  GAS unsigned* flags=(GAS unsigned*)(ws+WS_FLAGS);
  const u32x4 srdws=make_srd(ws, WS_SMALL);
  const int g=lane>>4, l15=lane&15, q=l15>>2, p=lane&3;
  P1 c; P2 d;
  unsigned selL=0x3f80u, selH=0x3f800000u; asm volatile("" : "+s"(selL), "+s"(selH));
  const int th=w>>1, fh=w&1;
  c.srdw=make_srd(th? wu:wg, D_*F_*2);
  { const int cc=lane&7; c.voff=(lane>>3)*9216+(((cc>>1)^((lane>>4)&3))*32)+(cc&1)*16; }
  c.m0base=ldsb+th*P1_REG+fh*6*1024;
  const unsigned rowoff=(unsigned)(fh*48)*9216u;
  auto unit_of=[&](int it){ return it<4 ? (isB?5:0)+((it+T)&3) : 4; };
  auto so_of=[&](int it){ return (unsigned)((xcd*9+unit_of(it))*128)+rowoff; };
  #define WST(lo,hi) if(w==1||w==2){ _Pragma("unroll 1") for(int s=lo;s<hi;s++) _Pragma("unroll") for(int j=0;j<12;j++) glds_m0(ldsb+th*P1_REG+s*P1_SLOT+j*1024, c.voff, c.srdw, so_of(0)-rowoff+(unsigned)(96*s+8*j)*9216u); }
  unsigned tag, vtag;
  const int h=lane>>5, blk=(lane>>4)&1; const unsigned vl=lane*16;
  const u32x4 srdo=make_srd(wo, D_*D_*2);
  unsigned sv[8]; u32x2 xr[5]; float mx;
  #define SLAB(lo,hi) _Pragma("unroll") for(int it=lo;it<hi;it++) slab_glds(ldsb+FR_SLAB+(w+4*it)*960, sv[it<20? it&3 : it-16], srdo, cg*144+(it<20?(it>>2)*108*1152:0));
  {
    const int tbA=tb, hg=cg, head=4*hg+w, off=2*(w&1);
    const u32x4 srdx=make_srd(x+(size_t)tbA*64*D_, 64*D_*2), srdw=make_srd(wqkv, D_*3*D_*2);
    const unsigned vx0=(lane>>3)*1152+(((lane&7)^(lane>>4))*16), vx1=(vx0^64)+8*1152;
    #pragma unroll
    for(int b=0;b<9;b++){ glds_m0(ldsb+w*2048+b*8192, vx0, srdx, w*16*1152+b*128); glds_m0(ldsb+w*2048+b*8192+1024, vx1, srdx, w*16*1152+b*128); }
    flag_load(vtag, flags+64*128+64);
    unsigned cw;
    { u64 pc; asm volatile("s_getpc_b64 %0" : "=s"(pc)); const u32x4 srdc=make_srd((const void*)(pc&~127ull), CODE_BYTES);
      bload32<0,false>(cw, (lane<13 && w==3)? lane*128 : 0x80000000u, srdc, i*13*128); }
    u32x4 wr0, wr1; const unsigned worw = w*256+blockIdx.x;
    bload128<0,false>(wr0, vl, srdo, worw*1152); bload128<1024,false>(wr1, lane<8? vl:0x80000000u, srdo, worw*1152);
    asm volatile("s_mov_b32 m0, %0\n\ts_nop 0\n\tbuffer_load_dwordx4 %1, %3, 0 offen lds\n\tbuffer_load_dword %2, %3, 0 offen offset:640 lds"
                 :: "s"(ldsb+K1_WPHI+3712+w*1024), "v"(lane<40? vl:0x80000000u), "v"(lane*4), "s"(make_srd(wphi+head*324, 648)) : "memory");
    unsigned wq[2];
    { const unsigned r15=divm<69,10>(lane), c15=lane-r15*15; const unsigned v=mad24(r15,3456,mad24(divm<52,8>(c15),512,c15*128))|(lane<WQW? 0:0x80000000u);
      bload32<0,false>(wq[0], v, srdw, (i*18+w*4)*3456+(xq&1)*512); bload32<0,false>(wq[1], (w==0 && lane<30)? v:0x80000000u, srdw, (i*18+16)*3456+(xq&1)*512); }
    unsigned vw[7];
    #pragma unroll
    for(int k=0;k<7;k++){ const unsigned ck=(w+4*k)*64+lane, row=divm<2428,16>(ck), cc=ck-__umul24(row,27); vw[k]=mad24(row,3456,mad24(divm<57,9>(cc),1008,cc*16)); }
    vw[6]|= (unsigned)(w+1)<<29&0x80000000u;
    const unsigned sw0=hg*144, mw0=ldsb+K1_RING+w*1024;
    #define A_W(PR,LO,HI) _Pragma("unroll") for(int k=LO;k<HI;k++) glds_m0(mw0+(PR)*K1_PAIR+k*4096, vw[k], srdw, sw0+(PR)*64*3456);
    STAMPIT(28);
    A_W(0,0,7)
    unsigned bx[4], bw[2];
    { const int row=lane&31;
      #pragma unroll
      for(int e=0;e<4;e++) bx[e]=ldsb+row*128+(((2*e+h)^((row>>1)&7))*16);
      const int hoff=36*w-2*off;
      #pragma unroll
      for(int t=0;t<2;t++){ unsigned gi=8*t+4*blk+p; gi=gi>14?14:gi; bw[t]=ldsb+K1_RING+mad24(8*h+q,432,mad24(divm<52,8>(gi),104,8*gi))+hoff; } }
    unsigned warm[2], wdst[2];
    { const unsigned r9=divm<57,9>(lane); warm[0]=mad24(r9,9216-1152,lane*128)|(lane<54? 0:0x80000000u); warm[1]= fh? 0x80000000u : warm[0]+6*9216; }
    f32x16 acc[2][2]={}; f32x4 cs4={}; float s1[2]={0,0}, s2[2]={0,0};
    u32x4 sel[2];
    #pragma unroll
    for(int t=0;t<2;t++){ const unsigned v= l15==2*t+(g&1)? 0x3f803f80u:0u; sel[t]=u32x4{v,v,v,v}; }
    unsigned wa[8];
    #pragma unroll
    for(int k=0;k<4;k++){ const unsigned idx=lane+64*k, dd=divm<683,13>(idx), fr=(idx-dd*12)*2, reg=(fr&3)+4*(fr>>3); const unsigned o=mad24(dd,36,((fr>>2)&1)*18+reg*2);
      wa[2*k]=ldsb+K1_WPHI+3712+w*1024+((reg<9 && dd<18)? o:648); wa[2*k+1]=ldsb+K1_WPHI+3712+w*1024+((reg+1<9 && dd<18)? o+2:648); }
    asm volatile("" : "+v"(acc[0][0]), "+v"(acc[0][1]), "+v"(acc[1][0]), "+v"(acc[1][1]), "+v"(cs4), "+v"(sel[0]), "+v"(sel[1]), "+v"(bx[0]), "+v"(bx[1]), "+v"(bx[2]), "+v"(bx[3]), "+v"(bw[0]), "+v"(bw[1]), "+v"(warm[0]), "+v"(warm[1]));
    asm volatile("" : "+v"(wa[0]), "+v"(wa[1]), "+v"(wa[2]), "+v"(wa[3]), "+v"(wa[4]), "+v"(wa[5]), "+v"(wa[6]), "+v"(wa[7]));
    A_W(1,0,7)
    asm volatile("s_waitcnt vmcnt(14)" : "+v"(vtag), "+v"(wr0), "+v"(wr1), "+v"(cw), "+v"(wq[0]), "+v"(wq[1]) :: "memory");
    {
      const unsigned a=ldsb+K1_WPHI+w*912+lane*4; unsigned v[4];
      #pragma unroll
      for(int k=0;k<4;k++) v[k]=*LDSP(u16,wa[2*k])|((unsigned)*LDSP(u16,wa[2*k+1])<<16);
      *LDSP(unsigned,a)=v[0]; *LDSP(unsigned,a+256)=v[1]; *LDSP(unsigned,a+512)=v[2]; if(lane<36) *LDSP(unsigned,a+768)=v[3]; }
    tag=rfl(vtag)*0x9E3779B1u+0x7F4A7C15u; vtag=tag; asm volatile("" : "+v"(vtag));
    { float s=0.f;
      #pragma unroll
      for(int k=0;k<4;k++){ s=dot2(wr0[k],0x3f803f80u,s); s=dot2(wr1[k],0x3f803f80u,s); }
      s=dpp_add<0xB1>(s); s=dpp_add<0x4E>(s); s=dpp_add<0x141>(s); s=dpp_add<0x140>(s); s=rows_sum(s);
      const unsigned hi=pk2(s,0.f)&0xffffu; const unsigned lo=pk2(s-lo2f(hi),0.f);
      const unsigned hd=(worw*3641u)>>16, dd=worw-hd*18;
      const unsigned a= dd<16? hd : 32+(hd>>3), hp= dd<16? dd>>3 : (hd>>2)&1, j= dd<16? dd&7 : 2*(hd&3)+(dd-16);
      bstore64_sc1(u32x2{hi|(lo<<16),vtag}, (lane || worw>=576)? 0x80000000u:0u, srdws, WS_WOROW+(32*(a>>1)+16*(j>>2)+4*(2*(a&1)+hp)+(j&3))*8); }
    u32x4 wq0,wq1,wk0,wk1;
    { const unsigned wb=ldsb+K1_WPHI+w*912+blk*32+p*8;
      auto wrow=[&](int dd){ return wb+((unsigned)dd<18u? dd:18)*48; };
      const int R=4*h+q;
      wk0=trfrag(wrow(R>=4? R-4-off:99),wrow(R+4-off)); wk1=trfrag(wrow(R+12-off),wrow(99));
      wq0=trfrag(wrow(R-off),wrow(R+8-off)); wq1=trfrag(wrow(R<4? R+16-off:99),wrow(99)); }
    A_W(2,0,3)
    asm volatile("s_waitcnt vmcnt(11) lgkmcnt(0)\n\ts_barrier" : "+v"(wq0), "+v"(wq1), "+v"(wk0), "+v"(wk1) :: "memory");
    STAMPIT(22);
    u32x4 xf[2][2][2], st[2]; u32x2 al[2][4], ah[2][4];
    const unsigned bst=ldsb+w*2048+vl;
    #define K1_X(B,E,TT,XB,E2) xf[B][E][TT]=*LDSP(u32x4,bx[2*(E2)+(E)]+(XB)*8192+(TT)*4096);
    #define K1_A(B,I,SL) { al[B][I]=trb(bw[(I)&1]+k1_slot(SL)+((I)>>1)*6912); ah[B][I]=trb(bw[(I)&1]+k1_slot(SL)+((I)>>1)*6912+1728); }
    K1_X(0,0,0,0,0) K1_X(0,0,1,0,0) K1_A(0,0,0) K1_A(0,1,0) K1_X(0,1,0,0,0) K1_X(0,1,1,0,0) K1_A(0,2,0) K1_A(0,3,0) st[0]=*LDSP(u32x4,bst);
    #define MM(D,I,B) SB(); mv32<false>(D,cat(al[cu][I],ah[cu][I]),B); SB();
    #define MC(D,I) SB(); mv16(cs4,sel[(I)&1],cat(al[cu][I],ah[cu][I])); SB();
    #define WL(K) if(cu==0 || (K)<3) glds_m0(mw0+(((j+5)%6)>>1)*K1_PAIR+((cu?0:3)+(K))*4096, vw[(cu?0:3)+(K)], srdw, sw);
    #define SD(K) s1[cu]=dot2(st[cu][K],0x3f803f80u,s1[cu]); s2[cu]=dot2(st[cu][K],st[cu][K],s2[cu]);
    #pragma unroll 1
    for(int s6=0;s6<18;s6+=6){
      #pragma unroll
      for(int j=0;j<6;j++){
        const int s=s6+j; const int cu=j&1, n=cu^1;
        const unsigned sw=sw0+((s+5)>>1)*64*3456;

        MM(acc[0][0],0,xf[cu][0][0])
        WL(0)
        MM(acc[0][1],0,xf[cu][0][1])
        K1_X(n,0,0,(s+1)>>1,n) K1_X(n,0,1,(s+1)>>1,n) SD(0)
        MC(cs[0],0)
        K1_A(n,0,(j+1)%6) SD(1)
        MM(acc[1][0],1,xf[cu][0][0])
        WL(1)
        MM(acc[1][1],1,xf[cu][0][1])
        K1_A(n,1,(j+1)%6) SD(2)
        MC(cs[1],1)
        K1_X(n,1,0,(s+1)>>1,n) K1_X(n,1,1,(s+1)>>1,n) SD(3)
        MM(acc[0][0],2,xf[cu][1][0])
        WL(2)
        MM(acc[0][1],2,xf[cu][1][1])
        K1_A(n,2,(j+1)%6)
        MC(cs[0],2)
        K1_A(n,3,(j+1)%6)
        MM(acc[1][0],3,xf[cu][1][0])
        WL(3)
        MC(cs[1],3)
        st[n]=*LDSP(u32x4,bst+((s+1)>>1)*8192+n*1024);
        MM(acc[1][1],3,xf[cu][1][1])
        asm volatile("s_waitcnt vmcnt(%0)\n\ts_barrier" :: "i"(cu? 10:7) : "memory");
      }
    }
    #undef MM
    #undef SD
    #undef K1_X
    #undef K1_A
    #undef WL
    #undef A_W
    STAMPIT(23);
    { const int row=w*16+l15;
      #pragma unroll
      for(int j=0;j<5;j++){ const unsigned cb=cg*144+32*j+8*(j<4? g:g&1); xr[j]=*LDSP(u32x2,ldsb+(cb>>7)*8192+row*128+((((cb>>4)&7)^((row>>1)&7))*16)+(cb&8)); } }
    asm volatile("s_waitcnt lgkmcnt(0)\n\ts_barrier" : "+v"(xr[0]), "+v"(xr[1]), "+v"(xr[2]), "+v"(xr[3]), "+v"(xr[4]) :: "memory");
    asm volatile("s_nop 7" : "+v"(cs4));
    if(lane<16){
      #pragma unroll
      for(int k=0;k<4;k++) *LDSP(float,ldsb+K1_STAT+512+w*256+k*64+lane*4)=cs4[k]; }
    #pragma unroll
    for(int j=0;j<2;j++){ float a=s1[j], b=s2[j];
      a=dpp_add<0xB1>(a); b=dpp_add<0xB1>(b); a=dpp_add<0x4E>(a); b=dpp_add<0x4E>(b); a=dpp_add<0x141>(a); b=dpp_add<0x141>(b);
      const float m=a*(1.f/D_), r=__builtin_amdgcn_rsqf(fmaxf(b*(1.f/D_)-m*m,0.f)+1e-5f);
      *LDSP(f32x2,ldsb+K1_STAT+(w*16+j*8+(lane>>3))*8)=f32x2{-m,r}; }
    asm volatile("s_waitcnt vmcnt(0) lgkmcnt(0)\n\ts_barrier" ::: "memory");
    bload32<0,false>(wdst[0], warm[0], c.srdw, xcd*1152+((T*2+isB)*18+fh*12)*9216+NOFW); bload32<0,false>(wdst[1], warm[1], c.srdw, xcd*1152+((T*2+isB)*18+fh*12)*9216+NOFW);
    asm volatile("" : "+v"(acc[0][0]), "+v"(acc[0][1]), "+v"(acc[1][0]), "+v"(acc[1][1]));
    f32x16 cs[2];
    #pragma unroll
    for(int t=0;t<2;t++)
      #pragma unroll
      for(int k=0;k<4;k++){ const f32x4 v=*LDSP(f32x4,ldsb+K1_STAT+512+w*256+t*128+k*32+h*16); cs[t][4*k]=v[0]; cs[t][4*k+1]=v[1]; cs[t][4*k+2]=v[2]; cs[t][4*k+3]=v[3]; }
    const f32x2 mr[2]={*LDSP(f32x2,ldsb+K1_STAT+(lane&31)*8), *LDSP(f32x2,ldsb+K1_STAT+256+(lane&31)*8)};
    mx=-*LDSP(float,ldsb+K1_STAT+(w*16+l15)*8);
    const unsigned PK=ldsb+w*10240, VV=PK+5120+64, KT=VV+3072;
    u32x4 f00[2], f01[2], f10[2]; f32x16 pk[2], pq[2];
    #pragma unroll
    for(int t=0;t<2;t++){
      acc[0][t]=__builtin_elementwise_fma(cs[0],(f32x16)mr[t][0],acc[0][t]); acc[1][t]=__builtin_elementwise_fma(cs[1],(f32x16)mr[t][0],acc[1][t]);
      f00[t]=packfrag(acc[0][t],0); f01[t]=packfrag(acc[0][t],1); f10[t]=packfrag(acc[1][t],0); }
    mz32(pk[0],wk0,f01[0]); mz32(pk[1],wk0,f01[1]); mv32<true>(pk[0],wk1,f10[0]); mv32<true>(pk[1],wk1,f10[1]);
    mz32(pq[0],wq0,f00[0]); mz32(pq[1],wq0,f00[1]); mv32<false>(pq[0],wq1,f01[0]); mv32<false>(pq[1],wq1,f01[1]);
    asm volatile("s_nop 7" : "+v"(pk[0]), "+v"(pk[1]));
    #pragma unroll
    for(int t=0;t<2;t++){
      unsigned ok[9]; hedgehog(pk[t],mr[t][1],ok);
      const int tok=32*t+(lane&31); const float r=mr[t][1]; const f32x16& v=acc[1][t];
      const unsigned pa=PK+tok*80+h*40;
      *LDSP(u32x2,pa)=u32x2{ok[0],ok[1]}; *LDSP(u32x2,pa+8)=u32x2{ok[2],ok[3]}; *LDSP(u32x2,pa+16)=u32x2{ok[4],ok[5]}; *LDSP(u32x2,pa+24)=u32x2{ok[6],ok[7]}; *LDSP(u32x2,pa+32)=u32x2{ok[8],0u};
      const unsigned va=VV+tok*48+h*8;
      *LDSP(u32x2,va)=u32x2{pk2(v[4]*r,v[5]*r),pk2(v[6]*r,v[7]*r)}; *LDSP(u32x2,va+16)=u32x2{pk2(v[8]*r,v[9]*r),pk2(v[10]*r,v[11]*r)};
      *LDSP(u32x2,va+32)= h? u32x2{0x3f803f80u,0x3f803f80u} : u32x2{pk2(v[12]*r,v[13]*r),pk2(v[14]*r,v[15]*r)}; }
    { f32x16 kv0, kv1;
      const unsigned pa=PK+(8*h+q)*80+blk*32+p*8, va=VV+(8*h+q)*48+blk*32+p*8;
      #pragma unroll
      for(int s=0;s<4;s++){ const u32x4 b=trfrag(va+s*768,va+s*768+192), a0=trfrag(pa+s*1280,pa+s*1280+320), a1=trfrag(pa+64+s*1280,pa+64+s*1280+320);
        if(s==0){ mz32(kv0,a0,b); mz32(kv1,a1,b); } else { mv32<false>(kv0,a0,b); mv32<false>(kv1,a1,b); } }
      MPAD(kv0,kv1);
      const int vc=lane&31, r19 = vc==20 ? 18 : vc-off; const unsigned dst=KT+r19*80+8*h;
      if((unsigned)(vc-off)<18u || vc==20){ *LDSP(u32x2,dst)=u32x2{pk2(kv0[0],kv0[1]),pk2(kv0[2],kv0[3])}; *LDSP(u32x2,dst+16)=u32x2{pk2(kv0[4],kv0[5]),pk2(kv0[6],kv0[7])};
        *LDSP(u32x2,dst+32)=u32x2{pk2(kv0[8],kv0[9]),pk2(kv0[10],kv0[11])}; *LDSP(u32x2,dst+48)=u32x2{pk2(kv0[12],kv0[13]),pk2(kv0[14],kv0[15])}; *LDSP(u32x2,dst+64)=u32x2{pk2(kv1[0],kv1[1]),pk2(kv1[2],kv1[3])}; }
      const u32x4 t0=*LDSP(u32x4,KT+vl), t1=*LDSP(u32x4,KT+1024+(vl&511));
      const unsigned so=WS_KVP+(head*32+tbA)*1536;
      bstore128_sc1(t0, vl, srdws, so); bstore128_sc1(t1, h? 0x80000000u:vl, srdws, so+1024); }
    STAMPIT(24);
    asm volatile("" : "+v"(pq[0]), "+v"(pq[1]));
    unsigned oq[2][9];
    #pragma unroll
    for(int t=0;t<2;t++) hedgehog(pq[t],mr[t][1],oq[t]);
    asm volatile("s_waitcnt vmcnt(0)" : "+v"(oq[0][8]), "+v"(oq[1][8]), "+v"(wdst[0]), "+v"(wdst[1])); flag_store(flags+(32+head)*128+64+tbA, vtag);
    #pragma unroll
    for(int t=0;t<2;t++){ const unsigned so=WS_FQ+(head*64+tbA*2+t)*2304;
      bstore128_sc1(u32x4{oq[t][0],oq[t][1],oq[t][2],oq[t][3]}, vl, srdws, so); bstore128_sc1(u32x4{oq[t][4],oq[t][5],oq[t][6],oq[t][7]}, vl, srdws, so+1024); bstore32_sc1(oq[t][8], lane*4, srdws, so+2048); }
    STAMPIT(25);
    WAITV(0);
    flag_store(flags+(32+head)*128+96+tbA, vtag);
    STAMPIT(26);
  }
  {
    const int head=(i&3)*8+xq, tr=i>>2;
    poll_all(flags+(32+head)*128+64, (lane&31)*4, w, tag);
    BAR();
    STAMPIT(27);
    u32x4 pv[16], q0[2], q1[2]; unsigned q2[2]; u32x2 wr[3];
    { const unsigned vp= (lane&31)<24? h*1536+(lane&31)*16 : 0x80000000u;
      #pragma unroll
      for(int k=0;k<8;k++){ bload128<0,true>(pv[2*k], vp, srdws, WS_KVP+head*49152+w*384+k*6144); bload128<3072,true>(pv[2*k+1], vp, srdws, WS_KVP+head*49152+w*384+k*6144); } }
    bload64<0,true>(wr[0],tid*8,srdws,WS_WOROW); bload64<2048,true>(wr[1],tid*8,srdws,WS_WOROW); bload64<0,true>(wr[2],(tid&63)*8,srdws,WS_WOROW+4096);
    { const int rr10=divm<103,10>(lane), cc10=lane-rr10*10;
      #pragma unroll
      for(int k=0;k<4;k++){ sv[k]=slab_voff<true>(w+4*k,rr10,cc10); sv[4+k]=slab_voff<false>(w+4*(20+k),rr10,cc10); asm volatile("" : "+v"(sv[k]), "+v"(sv[4+k])); } }
    { const int r=lane&31; const unsigned one= r<8? ((r&1)? 0x3f800000u:0x3f80u) : 0u;
      const u32x4 sel={(r>>1)==0?one:0u,(r>>1)==1?one:0u,(r>>1)==2?one:0u,(r>>1)==3?one:0u};
      f32x16 z;
      #pragma unroll
      for(int k=0;k<16;k++){ asm volatile("s_waitcnt vmcnt(%1)" : "+v"(pv[k]) : "i"(18-k)); if(k==0) mz32(z,sel,pv[0]); else mv32<false>(z,sel,pv[k]); }
      asm volatile("s_nop 7\n\ts_nop 3" : "+v"(z));
      const bool zero= w==3 && r>=23;
      if(r<24 || (w==3 && r<28)) *LDSP(u32x2,ldsb+FR_KVB+(24*w+r)*16+8*h)= zero? u32x2{0,0} : u32x2{pk2(z[0],z[1]),pk2(z[2],z[3])}; }
    STAMPIT(1);
    for(int n=0;n<0x100000;n++){
      asm volatile("s_waitcnt vmcnt(0)" : "+v"(wr[0]), "+v"(wr[1]), "+v"(wr[2]));
      if(__builtin_amdgcn_ballot_w64(wr[0][1]!=tag || wr[1][1]!=tag || wr[2][1]!=tag)==0) break;
      __builtin_amdgcn_s_sleep(2);
      bload64<0,true>(wr[0],tid*8,srdws,WS_WOROW); bload64<2048,true>(wr[1],tid*8,srdws,WS_WOROW); bload64<0,true>(wr[2],(tid&63)*8,srdws,WS_WOROW+4096); }
    asm volatile("s_waitcnt vmcnt(0)" : "+v"(wr[0]), "+v"(wr[1]), "+v"(wr[2]));
    { const unsigned a=ldsb+FR_SLAB+tid*160+144; *LDSP(u32x4,a)=u32x4{wr[0][0],0,0,0}; *LDSP(u32x4,a+256*160)=u32x4{wr[1][0],0,0,0}; if(tid<64) *LDSP(u32x4,a+512*160)=u32x4{wr[2][0],0,0,0}; }
    poll_all(flags+(32+head)*128+96+tr*4+w, 0, 0, tag);
    #pragma unroll
    for(int t=0;t<2;t++){ const unsigned so=WS_FQ+(head*64+tr*8+w*2+t)*2304; bload128<0,true>(q0[t],vl,srdws,so); bload128<1024,true>(q1[t],vl,srdws,so); bload32<2048,true>(q2[t],lane*4,srdws,so); }
    SLAB(0,SLA)
    asm volatile("s_waitcnt lgkmcnt(0)\n\ts_barrier" ::: "memory");
    STAMPIT(16);
    const int r=lane&31, hf=(r>>2)&1, reg=(r&3)+4*(r>>3); const int r19 = reg<8 ? 8*hf+reg : reg==10 ? 18 : (reg<10 && !hf) ? 8+reg : 19;
    const unsigned ap=ldsb+FR_KVB+r19*80+h*40;
    const u32x2 b0=*LDSP(u32x2,ap), b1=*LDSP(u32x2,ap+8), b2=*LDSP(u32x2,ap+16), b3=*LDSP(u32x2,ap+24); const u32x4 a0=cat(b0,b1), a1=cat(b2,b3); const unsigned a2=*LDSP(unsigned,ap+32);
    #pragma unroll
    for(int t=0;t<2;t++){ f32x16 z;
      asm volatile("s_waitcnt vmcnt(%3)" : "+v"(q0[t]), "+v"(q1[t]), "+v"(q2[t]) : "i"((t? 2:3)+SLA));
      mz32(z,a0,q0[t]); mv32<true>(z,a1,q1[t]); mv32<true>(z,u32x4{a2,0,0,0},u32x4{q2[t],0,0,0}); asm volatile("s_nop 7\n\ts_nop 3" : "+v"(z));
      const float rc=__builtin_amdgcn_rcpf(z[10]+1e-6f);
      const unsigned so=WS_OF+(tr*8+w*2+t)*36*1024;
      bstore128_sc1(u32x4{pk2(z[0]*rc,z[1]*rc),pk2(z[2]*rc,z[3]*rc),pk2(z[4]*rc,z[5]*rc),pk2(z[6]*rc,z[7]*rc)}, vl, srdws, so+head*1024);
      bstore32_sc1(pk2(z[8]*rc,z[9]*rc), h? 0x80000000u : vl, srdws, so+(32+(head>>3))*1024+((head>>2)&1)*512+(head&3)*4); }
    STAMPIT(17);
    SLAB(SLA,SLB)
    WAITV(SLB-SLA);
    flag_store(flags+(tr*4+w)*128+64+head, vtag);
    STAMPIT(2);
    SLAB(SLB,24)
    #undef SLAB
  }
  WST(0,2)
  poll_all(flags+tb*128+64, (lane&31)*4, w, tag);
  if(w==3) WAITV(0); else WAITV(24);
  BAR();
  if(blockIdx.x==0 && w==3) flag_store(flags+64*128+64, vtag);
  STAMPIT(3);
  {
    u32x4 o[18];
    { const unsigned vo=(g>>1)*1024+((g&1)*32+(w&1)*16+l15)*16, so=WS_OF+(tb*2+(w>>1))*36*1024;
      #define LD_(k) bload128<((k)&1)*2048,true>(o[k], vo, srdws, so+((k)>>1)*4096);
      LD_(0) LD_(1) LD_(2) LD_(3) LD_(4) LD_(5) LD_(6) LD_(7) LD_(8) LD_(9) LD_(10) LD_(11) LD_(12) LD_(13) LD_(14) LD_(15) LD_(16) LD_(17)
      #undef LD_
    }
    f32x4 acc[5];
    const unsigned wb=ldsb+FR_SLAB+(4*g+q)*160+p*8;
    u32x4 a[3][5];
    #define K2_RD(S) _Pragma("unroll") for(int j=0;j<5;j++) a[(S)%3][j]=trfrag(wb+(S)*5120+j*32, wb+(S)*5120+2560+j*32);
    K2_RD(0) K2_RD(1)
    #pragma unroll
    for(int S=0;S<18;S++){
      if(S+2<18) { K2_RD(S+2) }
      SB();
      asm volatile("s_waitcnt vmcnt(%1)" : "+v"(o[S]) : "i"(17-S));
      #pragma unroll
      for(int j=0;j<5;j++){ if(S==0) asm volatile("v_mfma_f32_16x16x32_bf16 %0, %1, %2, 0" : "=&v"(acc[j]) : "v"(a[0][j]), "v"(o[0])); else mv16(acc[j],a[S%3][j],o[S]); }
      SB(); }
    #undef K2_RD
    asm volatile("s_nop 7" : "+v"(acc[0]), "+v"(acc[1]), "+v"(acc[2]), "+v"(acc[3]), "+v"(acc[4]));
    BAR();
    STAMPIT(4);
    const float m=mx+row_bcast2(acc[4][0]+acc[4][1])*(1.f/D_);
    float s2=0.f;
    #pragma unroll
    for(int j=0;j<5;j++){ const float v0=acc[j][0]+(lo2f(xr[j][0])-m), v1=acc[j][1]+(hi2f(xr[j][0])-m), v2=acc[j][2]+(lo2f(xr[j][1])-m), v3=acc[j][3]+(hi2f(xr[j][1])-m);
      const int col=cg*72+16*j+4*g; const bool ok= j<4 || g<2;
      bstore64_sc1(u32x2{pk2(v0,v1),pk2(v2,v3)}, ok? (unsigned)((col>>5)*1024+(l15+16*((col>>2)&3))*16+((col>>4)&1)*8) : 0x80000000u, srdws, WS_HF+(tb*4+w)*18*1024);
      s2+= ok? v0*v0+v1*v1+v2*v2+v3*v3 : 0.f; }
    s2=rows_sum(s2);
    STAMPIT(19);
    bstore32_sc1(__float_as_uint(g? m:s2), g==0? (unsigned)(WS_SQ+((tb*8+cg)*64+l15*4+w)*4) : (g==1 && cg==0)? (unsigned)(WS_MB+(tb*64+w*16+l15)*4) : 0x80000000u, srdws, 0);
    WAITV(0);
    flag_store(flags+tb*128+96+cg*4+w, vtag);
    STAMPIT(5);
  }
  WST(2,6)
  #undef WST
  c.srdact=make_srd(ws+WS_ACT, S_*F_*2);
  const unsigned f2=2*(g&1)+(q>>1);
  const unsigned bfull=ldsb+(4*g+q)*128+(((2*fh)^f2)*32)+p*8, bhalf=ldsb+(4*g+q)*128+(((2*isB+fh)^f2)*32)+p*8;
  #pragma unroll
  for(int k=0;k<3;k++){ c.bA[k]=bfull+k*2*P1_SLOT; c.bB[k]=(bfull^32)+k*2*P1_SLOT; }
  c.stoff=lane*16+th*4096+fh*8192;
  const int fc=w&1, ch=isB;
  d.srdw=make_srd((const char*)wd+(size_t)xcd*576*1152-4096, 576*1152+4096);
  d.srda=make_srd(ws+WS_ACT+(size_t)(T*144+xcd*18)*8192-4096, 18*8192+4096);
  d.m0w=ldsb+w*P2_WREG; d.sowb=(unsigned)(8*w*1152); d.soab=(unsigned)(w*2048);
  auto kst=[&](int s){ if(s==12) return 8+isB; if(s==17) return 9-isB; const int z= s>12? s-1:s, pr=z>>1; const int who = pr<4 ? (pr&1) : (pr>=6); const int j = pr<4 ? (pr>>1) : 2+(pr&1);
    return 2*(((who^isB)?5:0)+((j+T)&3))+(z&1); };
  u64 tabA=0, tabB=0;
  #pragma unroll
  for(int s=1;s<6;s++) tabA|=(u64)kst(s)<<(5*(s-1));
  #pragma unroll
  for(int s=6;s<18;s++) tabB|=(u64)kst(s)<<(5*(s-6));
  asm volatile("" : "+s"(tabA), "+s"(tabB));
  d.sow=d.sowb+kst(0)*36864u; d.soa=d.soab+kst(0)*8192u; d.tab=tabA;
  d.myflag=flags+me*128; d.pflag=flags+(me^8)*128; d.tag=tag; d.vtag=vtag;
  poll_all(flags+T*256+96, (lane&31)*4+(lane>>5)*512, w, tag);
  BAR();
  STAMPIT(6);
  { u32x4 sv[2]; c.vl=lane*16; c.srdx=make_srd(ws+WS_HF+(size_t)(T*8+th*4)*18*1024, 4*18*1024);
    c.vwarm=(4096+((T*2+isB)*18+w*6)*1152+lane*128)|((lane<54 && w<3)? 0:0x80000000u);
    bload128<0,true>(sv[0], l15*16+g*512, srdws, WS_SQ+(T*2+th)*2048); bload128<256,true>(sv[1], l15*16+g*512, srdws, WS_SQ+(T*2+th)*2048);
    #define XL(k) p1_xload<k,0>(c); p1_xload<k,1>(c); p1_xload<k,2>(c); p1_xload<k,3>(c);
    XL(0) XL(1) XL(2) XL(3) XL(4) XL(5)
    #undef XL
    WAITV(12);
    asm volatile("" : "+v"(sv[0]), "+v"(sv[1]));
    #pragma unroll
    for(int t=0;t<4;t++){ const float v=rows_sum(__uint_as_float(sv[0][t])+__uint_as_float(sv[1][t]))*(1.f/D_)+1e-5f;
      c.ir2[t]=v; c.c1[t]=-1.4426950408889634f*__builtin_amdgcn_rsqf(v); asm volatile("" : "+v"(c.c1[t]), "+v"(c.ir2[t])); } }
  BAR();
  STAMPIT(7);
  #pragma unroll
  for(int k=0;k<2;k++){ trread<0>(c.flo[k][0],c.bA[0]+k*4096); trread<2048>(c.fhi[k][0],c.bA[0]+k*4096); trread<P1_REG>(c.flo[k][1],c.bA[0]+k*4096); trread<P1_REG+2048>(c.fhi[k][1],c.bA[0]+k*4096); }
  unsigned utab=unit_of(0)|unit_of(1)<<4|unit_of(2)<<8|unit_of(3)<<12|4<<16; const unsigned sob=xcd*9*128+rowoff, stb=(T*144+xcd*18)*8192;
  asm volatile("" : "+s"(utab));
  c.so_next=sob+((utab>>4)&15)*128;
  p1_unit<0>(c, d, 0);
  c.st_prev=stb+(utab&15)*16384; c.so_cur=c.so_next; utab>>=4;
  STAMPIT(15);
  #pragma unroll 1
  for(int it=1;it<4;it++){
    const unsigned un=utab&15; utab>>=4;
    c.so_next=sob+(utab&15)*128; d.f1=d.myflag+(utab>>4? 2:0);
    p1_unit<1>(c, d, bfull^(it==3? bfull^bhalf : 0));
    c.st_prev=stb+un*16384; c.so_cur=c.so_next;
    STAMPV(it==1? 18:30);
  }
  asm volatile("" :: "v"(c.vwarm));
  STAMPIT(8);
  int ln2=threadIdx.x&63; asm volatile("" : "+v"(ln2));
  { const int cc=ln2&7; const unsigned v0=(ln2>>3)*1152+(((cc>>1)^((ln2>>4)&3))*32)+(cc&1)*16+ch*640+4096;
    #pragma unroll
    for(int j=0;j<4;j++) d.vw[j]=v0-j*896;
    const int c4=ln2&3, r=(ln2>>2)&7; d.vw[4]= ln2<32? r*1152+512+ch*64+(((c4>>1)^((r>>2)&1))*32)+(c4&1)*16+4096 : 0x80000000u;
    d.va=ln2*16+4096-512; }
  p1_unit<2>(c, d, 0);
  c.st_prev=(unsigned)((T*144+xcd*18+8+isB)*8192);
  STAMPIT(9);
  #pragma unroll
  for(int j=0;j<7;j++) p2_issue<4>(d,j);
  p2_next(d); d.tab=tabB;
  { asm volatile("" : "+v"(ln2)); const int lane=ln2, q=(lane&15)>>2, p=lane&3; const int g=lane>>4; const int f=2*(g&1)+(q>>1);
    c.stoff=lane*16+th*4096+fh*8;
    #pragma unroll
    for(int sp=0;sp<3;sp++){
      #pragma unroll
      for(int jj=0;jj<4;jj++) d.ba[sp][jj]=ldsb+sp*2*P2_SLOT+(g>>1)*P2_WREG+fc*2048+(4*(g&1)+q)*128+8*p+((jj^f)*32);
      d.ba[sp][4]=ldsb+sp*2*P2_SLOT+(g>>1)*P2_WREG+4096+(4*(g&1)+q)*64+8*p+((fc^(g&1))*32);
      d.bb[sp]=ldsb+sp*2*P2_SLOT+2*th*P2_WREG+4608+lane*16; } }
  ldsread128a<0>(d.bf[0][0],d.bb[0]); ldsread128a<1024>(d.bf[0][1],d.bb[0]); ldsread128a<P2_WREG>(d.bf[0][2],d.bb[0]); ldsread128a<P2_WREG+1024>(d.bf[0][3],d.bb[0]);
  trread<0>(d.flo[0],d.ba[0][0]); trread<2*P2_WREG>(d.fhi[0],d.ba[0][0]); trread<0>(d.flo[1],d.ba[0][1]); trread<2*P2_WREG>(d.fhi[1],d.ba[0][1]); trread<0>(d.flo[2],d.ba[0][2]); trread<2*P2_WREG>(d.fhi[2],d.ba[0][2]);
  p2_stage<0,true>(d,c);
  STAMPIT(29);
  #pragma unroll 1
  for(int s=0;s<2;s++){ d.f2=d.pflag+s; p2_stage<1,false>(d,c); p2_stage<2,false>(d,c); p2_stage<3,false>(d,c); p2_stage<4,false>(d,c); p2_stage<5,false>(d,c); p2_stage<0,false>(d,c); STAMPV(s? 30:31); }
  STAMPIT(10);
  asm volatile("" : "+v"(ln2));
  { const int lane=ln2, l15=lane&15, g=lane>>4;
  d.srdp=make_srd(ws+WS_PART, 8*S_*D_*2); d.vl=lane*16; d.ownl=ldsb+lane*16;
  #pragma unroll
  for(int u=0;u<2;u++){     unsigned o; asm volatile("s_cmp_eq_u32 %2, %3\n\ts_cselect_b32 %0, 0x80000000, %4\n\ts_cselect_b32 %1, 0, 9216" : "=&s"(d.pb[u]), "=&s"(o) : "s"(2*w+u), "s"(xcd), "s"((unsigned)(((((T*2+ch)*8+2*w+u)*8+xcd)*9)*1024)) : "scc");
    d.ownw[u]=d.ownl+o; }
  p2_tgroup<0>(d); p2_tgroup<1>(d);
  const int ow=xcd>>1, ou=xcd&1; const int t16=T*8+(ow>>1)*4+2*ou;
  u32x4 hr[2]; unsigned h8, mA, m8;
  const int ks=ch*10+(ow&1)*4+w;
  bload128<0,true>(hr[0], d.vl, srdws, WS_HF+(t16*18+ks)*1024); bload128<0,true>(hr[1], d.vl, srdws, WS_HF+((t16+1)*18+ks)*1024);
  bload32<0,true>(h8, ((lane>>1)&1)*18*1024+(16*w+(lane>>2))*16+(lane&1)*4, srdws, WS_HF+(t16*18+8+ch)*1024+(ow&1)*8);
  bload32<0,true>(mA, l15*4+(lane>>5)*64, srdws, WS_MB+t16*64); bload32<0,true>(m8, (((lane>>1)&1)*16+(lane>>2))*4, srdws, WS_MB+t16*64);
  p2_tgroup<2>(d);
  STAMPIT(11);
  asm volatile("s_nop 7" ::: "memory");
  p2_dump_all<0>(d);
  asm volatile("s_waitcnt vmcnt(0) lgkmcnt(0)" ::: "memory");
  asm volatile("s_mov_b64 exec, 3\n\tglobal_store_dword %0, %1, %2 sc1\n\ts_mov_b64 exec, -1" :: "v"(lane*512), "v"(d.vtag), "s"(flags+(((me&~7)+2*w)*128+32+xcd)) : "memory");
  STAMPIT(12);
  BAR();
  asm volatile("" : "+v"(hr[0]), "+v"(hr[1]), "+v"(h8), "+v"(mA), "+v"(m8));
  f32x16 z[2]; float s8[2]; u32x4 idn;
  { const int r=lane&31; const unsigned one= (r>>3)==(lane>>5)? ((r&1)? 0x3f800000u:0x3f80u) : 0u; const int e=(r&7)>>1; idn=u32x4{e==0?one:0u,e==1?one:0u,e==2?one:0u,e==3?one:0u};
    z[0]=(f32x16)__uint_as_float(mA); z[1]=z[0];
    mv32<true>(z[0],idn,u32x4{hr[0][0],hr[0][1],hr[1][0],hr[1][1]}); mv32<true>(z[1],idn,u32x4{hr[0][2],hr[0][3],hr[1][2],hr[1][3]});
    s8[0]=lo2f(h8)+__uint_as_float(m8); s8[1]=hi2f(h8)+__uint_as_float(m8); }
  u32x4 v[8][2]; unsigned v8[8];
  #pragma unroll
  for(int s=1;s<8;s++) asm volatile("" : "=v"(v[s][0]), "=v"(v[s][1]), "=v"(v8[s]));
  { const unsigned sb=(unsigned)(((T*2+ch)*8+xcd)*8*9*1024);
    unsigned todo=0xfe;
    for(int n=0; n<0x100000 && todo; n++){
      unsigned f; asm volatile("global_load_dword %0, %1, %2 sc1\n\ts_waitcnt vmcnt(0)" : "=v"(f) : "v"(((lane+xcd)&7)*4), "s"(flags+me*128+32) : "memory");
      const unsigned go= n<0xfffff? (unsigned)__builtin_amdgcn_ballot_w64(f==tag)&todo : todo;
      #pragma unroll
      for(int s=1;s<8;s++) if(go>>s&1){ const unsigned so=sb+((xcd+s)&7)*9*1024;
        asm volatile("buffer_load_dwordx4 %0, %3, %5, %6 offen sc1\n\tbuffer_load_dwordx4 %1, %3, %5, %6 offen offset:1024 sc1\n\tbuffer_load_dword %2, %4, %5, %7 offen sc1"
                     : "+v"(v[s][0]), "+v"(v[s][1]), "+v"(v8[s]) : "v"(d.vl), "v"(lane*4), "s"(d.srdp), "s"(so+w*2048), "s"(so+8192+w*256) : "memory"); }
      todo&=~go; }
    STAMPIT(13);
    v[0][0]=*LDSP(u32x4,d.ownl+w*2048); v[0][1]=*LDSP(u32x4,d.ownl+w*2048+1024); v8[0]=*LDSP(unsigned,ldsb+8192+w*256+lane*4); }
  STAMPIT(20);
  #pragma unroll
  for(int x_=0;x_<8;x_++){
    if(x_) asm volatile("s_waitcnt vmcnt(0)" : "+v"(v[x_][0]), "+v"(v[x_][1]), "+v"(v8[x_]));
    mv32<false>(z[0],idn,v[x_][0]); mv32<false>(z[1],idn,v[x_][1]);
    s8[0]=dot2(v8[x_],selL,s8[0]); s8[1]=dot2(v8[x_],selH,s8[1]); }
  MPAD(z[0],z[1]);
  STAMPIT(21);
  { u16* o=out+(size_t)(t16*16+l15+16*(lane>>5))*D_+ks*32+4*(g&1);
    #pragma unroll
    for(int k=0;k<2;k++){ OUTST(u32x2,o+16*k,(u32x2{pk2(z[k][0],z[k][1]),pk2(z[k][2],z[k][3])})); OUTST(u32x2,o+16*k+8,(u32x2{pk2(z[k][4],z[k][5]),pk2(z[k][6],z[k][7])})); }
    OUTST(unsigned,out+(size_t)(t16*16+((lane>>1)&1)*16+(lane>>2))*D_+256+32*ch+16*(ow&1)+4*w+2*(lane&1),pk2(s8[0],s8[1])); }
  }
  STAMPIT(14); STAMP_END(15);
}
extern "C" void kernel_launch(void* const* d_in, const int* in_sizes, int n_in, void* d_out, int out_size, void* d_ws, size_t ws_size, hipStream_t stream){
  const u16* x=(const u16*)d_in[0]; const u16* wqkv=(const u16*)d_in[1]; const u16* wphi=(const u16*)d_in[2]; const u16* wo=(const u16*)d_in[3];
  const u16* wg=(const u16*)d_in[5]; const u16* wu=(const u16*)d_in[6]; const u16* wd=(const u16*)d_in[7];
  char* ws=(char*)d_ws+((ws_size-WS_END)&~(size_t)0xfffff);
  (void)hipFuncSetAttribute((const void*)k3, hipFuncAttributeMaxDynamicSharedMemorySize, K_LDS);
  k3<<<256,256,K_LDS,stream>>>(x,wqkv,wphi,wo,wg,wu,wd,ws,(u16*)d_out STAMP_PASS(0));
}
```

```cpp
#include <hip/hip_runtime.h>
#include <stdint.h>
#include <utility>
typedef unsigned short u16;
typedef unsigned long long u64;
using u32x2=__attribute__((ext_vector_type(2)))unsigned;
using u32x4=__attribute__((ext_vector_type(4)))unsigned;
using f32x4=__attribute__((ext_vector_type(4)))float;
using f32x16=__attribute__((ext_vector_type(16)))float;
#define DI __device__ __forceinline__
constexpr int S_=2048, D_=576, H_=32, DH_=18, F_=4608;
DI float bf2f(u16 v){ return __uint_as_float(((unsigned)v)<<16); }
DI u16 f2bf(float f){ unsigned u=__float_as_uint(f); u += 0x7fff + ((u>>16)&1); return (u16)(u>>16); }
template<unsigned M,int SH> DI unsigned divm(unsigned x){ return __umul24(x,M)>>SH; }
DI unsigned mad24(unsigned a,unsigned b,unsigned c){ return __umul24(a,b)+c; }
DI unsigned rfl(unsigned x){ return __builtin_amdgcn_readfirstlane(x); }
DI u32x4 make_srd(const void* p, unsigned bytes){ u64 a=(u64)p; u32x4 s; s[0]=rfl((unsigned)a); s[1]=rfl((unsigned)(a>>32)&0xffffu); s[2]=bytes; s[3]=0x20000u; return s; }
#define WAITL(n) __builtin_amdgcn_s_waitcnt(0xC07F|((n)<<8))
#define WAITV(n) __builtin_amdgcn_s_waitcnt(0x0F70|((n)&15)|(((n)>>4)<<14))
#define BAR() asm volatile("s_barrier" ::: "memory")
template<int OFF> DI void trread(u32x2& d, unsigned addr){ asm volatile("ds_read_b64_tr_b16 %0, %1 offset:%2" : "=v"(d) : "v"(addr), "i"(OFF)); }
template<int OFF> DI void ldsread128(u32x4& d, unsigned addr){ asm volatile("ds_read_b128 %0, %1 offset:%2" : "=v"(d) : "v"(addr), "i"(OFF)); }
template<int OFF> DI void ldsread128f(f32x4& d, unsigned addr){ asm volatile("ds_read_b128 %0, %1 offset:%2" : "=v"(d) : "v"(addr), "i"(OFF)); }
DI u32x4 cat(const u32x2& a,const u32x2& b){ return __builtin_shufflevector(a,b,0,1,2,3); }
DI void glds_m0(unsigned m0, unsigned voff, u32x4 srd, unsigned soff){
  asm volatile("s_mov_b32 m0, %0\n\ts_nop 0\n\tbuffer_load_dwordx4 %1, %2, %3 offen lds" :: "s"(m0), "v"(voff), "s"(srd), "s"(soff) : "memory");
}
template<int OFF> DI void glds_off(unsigned voff, u32x4 srd, unsigned soff){
  asm volatile("buffer_load_dwordx4 %0, %1, %2 offen offset:%3 lds" :: "v"(voff), "s"(srd), "s"(soff), "i"(OFF) : "memory");
}
DI void bstore128(u32x4 v, unsigned voff, u32x4 srd, unsigned soff){
  asm volatile("buffer_store_dwordx4 %0, %1, %2, %3 offen\n\ts_nop 1" :: "v"(v), "v"(voff), "s"(srd), "s"(soff) : "memory");
}
DI void vfma(float& d, float a, float b, float c){ asm volatile("v_fma_f32 %0, %1, %2, %3" : "=v"(d) : "v"(a), "v"(b), "v"(c)); }
DI void vmul(float& d, float a, float b){ asm volatile("v_mul_f32 %0, %1, %2" : "=v"(d) : "v"(a), "v"(b)); }
DI void vexp(float& d){ asm volatile("v_exp_f32 %0, %0" : "+v"(d)); }
DI void vrcp(float& d){ asm volatile("v_rcp_f32 %0, %0" : "+v"(d)); }
DI void vcvtpk(unsigned& d, float a, float b){ asm volatile("v_cvt_pk_bf16_f32 %0, %1, %2" : "=v"(d) : "v"(a), "v"(b)); }

#define STAMP_ARG
#define STAMP_PASS(k)
#define STAMP_DECL
#define STAMPIT(i)
#define STAMPV(i)
#define STAMP_END(n)
constexpr unsigned WS_FQ=0, WS_KVP=WS_FQ+64*32*2304, WS_OF=WS_KVP+32*32*1536, WS_HF=WS_OF+S_*D_*2, WS_SQ=WS_HF+S_*D_*2, WS_MB=WS_SQ+S_*8*4, WS_MEANX=WS_MB+S_*4,
  WS_WOROW=WS_MEANX+S_*4, WS_SMALL=WS_WOROW+8192, WS_FLAGS=WS_SMALL, WS_ACT=WS_FLAGS+256*512, WS_PART=WS_ACT+S_*F_*2, WS_END=WS_PART+8*S_*D_*2;
using s16x4=__attribute__((ext_vector_type(4)))short;
using bf16x8=__attribute__((ext_vector_type(8)))__bf16;
using f32x2=__attribute__((ext_vector_type(2)))float;
using bf16x2=__attribute__((ext_vector_type(2)))__bf16;
#define LDSP(T,a) ((__attribute__((address_space(3))) T*)(a))
DI u32x2 trb(unsigned a){ return __builtin_bit_cast(u32x2, __builtin_amdgcn_ds_read_tr16_b64_v4i16(LDSP(s16x4,a))); }
DI u32x4 trfrag(unsigned a0, unsigned a1){ return cat(trb(a0),trb(a1)); }
DI f32x16 mfma32(u32x4 a,u32x4 b,f32x16 c){ return __builtin_amdgcn_mfma_f32_32x32x16_bf16(__builtin_bit_cast(bf16x8,a),__builtin_bit_cast(bf16x8,b),c,0,0,0); }
DI f32x4 mfma16(u32x4 a,u32x4 b,f32x4 c){ return __builtin_amdgcn_mfma_f32_16x16x32_bf16(__builtin_bit_cast(bf16x8,a),__builtin_bit_cast(bf16x8,b),c,0,0,0); }
DI unsigned pk2(float lo,float hi){ f32x2 v={lo,hi}; return __builtin_bit_cast(unsigned,__builtin_convertvector(v,bf16x2)); }
DI float lo2f(unsigned v){ return __uint_as_float(v<<16); }
DI float hi2f(unsigned v){ return __uint_as_float(v&0xffff0000u); }
DI float xhalf_max(float v){ auto r=__builtin_amdgcn_permlane32_swap(__float_as_uint(v),__float_as_uint(v),false,false); return fmaxf(__uint_as_float(r[0]),__uint_as_float(r[1])); }
DI float xhalf_min(float v){ auto r=__builtin_amdgcn_permlane32_swap(__float_as_uint(v),__float_as_uint(v),false,false); return fminf(__uint_as_float(r[0]),__uint_as_float(r[1])); }
DI float xhalf_sum(float v){ auto r=__builtin_amdgcn_permlane32_swap(__float_as_uint(v),__float_as_uint(v),false,false); return __uint_as_float(r[0])+__uint_as_float(r[1]); }
DI u32x4 packfrag(const f32x16& a,int s){ return u32x4{pk2(a[8*s],a[8*s+1]),pk2(a[8*s+2],a[8*s+3]),pk2(a[8*s+4],a[8*s+5]),pk2(a[8*s+6],a[8*s+7])}; }
DI void hedgehog(const f32x16& p, float r, unsigned (&o)[9]){
  float mx=fmaxf(fmaxf(p[0],p[1]),p[2]), mn=fminf(fminf(p[0],p[1]),p[2]);
  #pragma unroll
  for(int i=3;i<9;i+=2){ mx=fmaxf(fmaxf(mx,p[i]),p[i+1]); mn=fminf(fminf(mn,p[i]),p[i+1]); }
  mx=xhalf_max(mx); mn=xhalf_min(mn);
  const float L=1.4426950408889634f*r; const f32x2 LL={L,-L}, ab={-mx*L,mn*L};
  f32x2 e[9], s={0.f,0.f};
  #pragma unroll
  for(int i=0;i<9;i++){ const f32x2 t=__builtin_elementwise_fma(f32x2{p[i],p[i]},LL,ab); e[i]=f32x2{__builtin_amdgcn_exp2f(t[0]),__builtin_amdgcn_exp2f(t[1])}; s= i? s+e[i] : e[i]; }
  s=f32x2{__builtin_amdgcn_rcpf(xhalf_sum(s[0])),__builtin_amdgcn_rcpf(xhalf_sum(s[1]))};
  #pragma unroll
  for(int i=0;i<9;i++){ const f32x2 t=e[i]*s; o[i]=pk2(t[0],t[1]); }
}
template<bool P> DI void mv32(f32x16& d, const u32x4& a, const u32x4& b){
  if constexpr (P) asm volatile("s_nop 1\n\tv_mfma_f32_32x32x16_bf16 %0, %1, %2, %0" : "+v"(d) : "v"(a), "v"(b)); else asm volatile("v_mfma_f32_32x32x16_bf16 %0, %1, %2, %0" : "+v"(d) : "v"(a), "v"(b)); }
DI void mz32(f32x16& d, const u32x4& a, const u32x4& b){ asm volatile("s_nop 1\n\tv_mfma_f32_32x32x16_bf16 %0, %1, %2, 0" : "=&v"(d) : "v"(a), "v"(b)); }
#define MPAD(a,b) asm volatile("s_nop 7\n\ts_nop 3" : "+v"(a), "+v"(b))
template<int CTRL> DI float dpp_add(float v){ return v+__builtin_bit_cast(float,__builtin_amdgcn_update_dpp(0,__builtin_bit_cast(int,v),CTRL,0xf,0xf,true)); }
DI float dot2(unsigned a,unsigned b,float c){ return __builtin_amdgcn_fdot2_f32_bf16(__builtin_bit_cast(bf16x2,a),__builtin_bit_cast(bf16x2,b),c,false); }
constexpr int K1_RING=73728, K1_SLOT=13824, K1_PAIR=28672, K1_WPHI=K1_RING+2*K1_PAIR+K1_SLOT+2048, K1_STAT=49152, K_LDS=K1_RING+3*K1_PAIR;
constexpr int k1_slot(int s){ return (s>>1)*K1_PAIR+(s&1)*K1_SLOT; }
DI void mv16(f32x4& d, const u32x4& a, const u32x4& b){ asm volatile("v_mfma_f32_16x16x32_bf16 %0, %1, %2, %0" : "+v"(d) : "v"(a), "v"(b)); }
#define SB() __builtin_amdgcn_sched_barrier(0)
constexpr int P1_SLOT=24576, P1_REG=12288;
constexpr int P2_WREG=6656, P2_SLOT=4*P2_WREG;
constexpr int FR_SLAB=53248, FR_KVB=FR_SLAB+576*160;
#define MAP 1
#define CODE_BYTES 46000
#define WQW 60
#define NOFW 0
#define SLA 4
#define SLB 12
#define OUTST(T,p,v) *(T*)(p)=v
#define GAS __attribute__((address_space(1)))
struct P1 {
  u32x4 x[18][4];
  f32x4 acc[2][2][4];
  float tz[2][4][4], tp[2][4][4];
  u32x2 flo[3][2], fhi[3][2];
  unsigned pk[2][4];
  float c1[4], ir2[4];
  unsigned bA[3], bB[3];
  unsigned voff, stoff, vl, vwarm;
  u32x4 srdw, srdact, srdx;
  unsigned m0base, so_cur, so_next, st_prev;
};
struct P2 {
  f32x4 acc[9][4];
  u32x2 flo[6], fhi[6];
  u32x4 bf[2][4], bt[4][4];
  unsigned ba[3][5], bb[3];
  unsigned vw[5], va;
  u32x4 srdw, srda, srdp;
  unsigned m0w, sowb, soab, sow, soa, kk;
  u64 tab;
  GAS unsigned* myflag; GAS unsigned* pflag; GAS unsigned* f1; GAS unsigned* f2; unsigned pv, tag, vtag;
  unsigned r[4], vl, ownl, pb[2], ownw[2];
};
DI void bstore128_sc1(u32x4 v, unsigned voff, u32x4 srd, unsigned soff){
  asm volatile("buffer_store_dwordx4 %0, %1, %2, %3 offen sc1\n\ts_nop 1" :: "v"(v), "v"(voff), "s"(srd), "s"(soff) : "memory");
}
DI void bstore64_sc1(u32x2 v, unsigned voff, u32x4 srd, unsigned soff){
  asm volatile("buffer_store_dwordx2 %0, %1, %2, %3 offen sc1" :: "v"(v), "v"(voff), "s"(srd), "s"(soff) : "memory");
}
DI void bstore32_sc1(unsigned v, unsigned voff, u32x4 srd, unsigned soff){
  asm volatile("buffer_store_dword %0, %1, %2, %3 offen sc1" :: "v"(v), "v"(voff), "s"(srd), "s"(soff) : "memory");
}
template<int OFF> DI void glds_off_sc1(unsigned voff, u32x4 srd, unsigned soff){
  asm volatile("buffer_load_dwordx4 %0, %1, %2 offen offset:%3 sc1 lds" :: "v"(voff), "s"(srd), "s"(soff), "i"(OFF) : "memory");
}
template<int OFF> DI void set_m0(unsigned base){ asm volatile("s_add_u32 m0, %0, %1" :: "s"(base), "i"(OFF) : "scc", "memory"); }
template<int OFF> DI void glds_half(unsigned voff, u32x4 srd, unsigned soff){
  asm volatile("buffer_load_dwordx4 %0, %1, %2 offen offset:%3 lds" :: "v"(voff), "s"(srd), "s"(soff), "i"(OFF) : "memory");
}
template<int OFF> DI void ldsread128a(u32x4& d, unsigned addr){ asm volatile("ds_read_b128 %0, %1 offset:%2" : "=a"(d) : "v"(addr), "i"(OFF)); }
template<int OFF,bool SC1> DI void bload128(u32x4& d, unsigned voff, u32x4 srd, unsigned soff){
  if constexpr (SC1) asm volatile("buffer_load_dwordx4 %0, %1, %2, %3 offen offset:%4 sc1" : "=v"(d) : "v"(voff), "s"(srd), "s"(soff), "i"(OFF) : "memory");
  else asm volatile("buffer_load_dwordx4 %0, %1, %2, %3 offen offset:%4" : "=v"(d) : "v"(voff), "s"(srd), "s"(soff), "i"(OFF) : "memory");
}
template<int OFF,bool SC1> DI void bload64(u32x2& d, unsigned voff, u32x4 srd, unsigned soff){
  if constexpr (SC1) asm volatile("buffer_load_dwordx2 %0, %1, %2, %3 offen offset:%4 sc1" : "=v"(d) : "v"(voff), "s"(srd), "s"(soff), "i"(OFF) : "memory");
  else asm volatile("buffer_load_dwordx2 %0, %1, %2, %3 offen offset:%4" : "=v"(d) : "v"(voff), "s"(srd), "s"(soff), "i"(OFF) : "memory");
}
template<int OFF,bool SC1> DI void bload32(unsigned& d, unsigned voff, u32x4 srd, unsigned soff){
  if constexpr (SC1) asm volatile("buffer_load_dword %0, %1, %2, %3 offen offset:%4 sc1" : "=v"(d) : "v"(voff), "s"(srd), "s"(soff), "i"(OFF) : "memory");
  else asm volatile("buffer_load_dword %0, %1, %2, %3 offen offset:%4" : "=v"(d) : "v"(voff), "s"(srd), "s"(soff), "i"(OFF) : "memory");
}
DI void flag_store(GAS unsigned* p, unsigned v){
  asm volatile("global_store_dword %0, %1, %2 sc1" :: "v"(0u), "v"(v), "s"(p) : "memory");
}
DI void flag_load(unsigned& d, GAS unsigned* p){ asm volatile("global_load_dword %0, %1, %2 sc1" : "=v"(d) : "v"(0u), "s"(p) : "memory"); }
DI void flag_check(unsigned& v, GAS unsigned* p, unsigned need){
  unsigned s_, n_;
  asm volatile("s_nop 0\n\tv_readfirstlane_b32 %0, %2\n\ts_cmp_eq_u32 %0, %5\n\ts_cbranch_scc1 .Ldone%=\n\ts_mov_b32 %1, 0x100000\n"
               ".Lspin%=:\n\ts_sleep 2\n\tglobal_load_dword %2, %3, %4 sc1\n\ts_waitcnt vmcnt(0)\n\tv_readfirstlane_b32 %0, %2\n\ts_cmp_eq_u32 %0, %5\n\ts_cbranch_scc1 .Ldone%=\n\t"
               "s_sub_u32 %1, %1, 1\n\ts_cbranch_scc0 .Lspin%=\n.Ldone%=:"
               : "=&s"(s_), "=&s"(n_), "+&v"(v) : "v"(0u), "s"(p), "s"(need) : "scc", "memory");
}
DI void poll_all(GAS unsigned* p, unsigned voff, unsigned skip, unsigned tag){
  unsigned v_, n_;
  asm volatile("s_cmp_lg_u32 %4, 0\n\ts_cbranch_scc1 .Ldone%=\n\ts_mov_b32 %1, 0x100000\n"
               ".Lspin%=:\n\tglobal_load_dword %0, %2, %3 sc1\n\ts_waitcnt vmcnt(0)\n\tv_cmp_ne_u32 vcc, %5, %0\n\ts_cbranch_vccz .Ldone%=\n\t"
               "s_sleep 1\n\ts_sub_u32 %1, %1, 1\n\ts_cbranch_scc0 .Lspin%=\n.Ldone%=:"
               : "=&v"(v_), "=&s"(n_) : "v"(voff), "s"(p), "s"(skip), "s"(tag) : "scc", "vcc", "memory");
}
template<int Z,bool AG> DI void p1_mfma(f32x4& acc, const u32x4& a, const u32x4& x){
  if constexpr (Z==2)  asm volatile("v_mfma_f32_16x16x32_bf16 %0, %1, %2, 0" : "=&v"(acc) : "v"(a), "a"(x));
  if constexpr (Z==1 && AG)  asm volatile("v_mfma_f32_16x16x32_bf16 %0, %1, %2, 0" : "+v"(acc) : "v"(a), "a"(x));
  if constexpr (!Z && AG) asm volatile("v_mfma_f32_16x16x32_bf16 %0, %1, %2, %0" : "+v"(acc) : "v"(a), "a"(x));
  if constexpr (!AG)      asm volatile("v_mfma_f32_16x16x32_bf16 %0, %1, %2, %0" : "+v"(acc) : "v"(a), "v"(x));
}
template<int TL,int I> DI void p1_dr(P1& c){
  constexpr int tt=(I>>2)&3, r=I&3;
  if constexpr (I<16) vmul(c.tz[TL][tt][r], c.acc[TL][0][tt][r], c.c1[tt]); else vmul(c.tp[TL][tt][r], c.acc[TL][0][tt][r], c.acc[TL][1][tt][r]);
}
constexpr int dr_cum(int G){ int n=0; for(int g=0;g<G;g++){ const int m=g&7; n+= m==6?0 : (m&1)?1:2; if(g==1||g==9) n++; } return n; }
template<int TL,int LO,int HI> DI void p1_drs(P1& c){ if constexpr (LO<HI){ p1_dr<TL,LO>(c); p1_drs<TL,LO+1,HI>(c);} }
template<int TT,int HF,int G> DI void p1_grp(P1& c){
  float* z=c.tz[HF][TT]; float* p=c.tp[HF][TT];
  if constexpr (G==0) vexp(z[0]); if constexpr (G==2) vexp(z[1]); if constexpr (G==4) vexp(z[2]); if constexpr (G==8) vexp(z[3]);
  if constexpr (G==5) vfma(z[0],z[0],c.ir2[TT],c.ir2[TT]); if constexpr (G==7) vfma(z[1],z[1],c.ir2[TT],c.ir2[TT]);
  if constexpr (G==9) vfma(z[2],z[2],c.ir2[TT],c.ir2[TT]); if constexpr (G==13) vfma(z[3],z[3],c.ir2[TT],c.ir2[TT]);
  if constexpr (G==10) vrcp(z[0]); if constexpr (G==12) vrcp(z[1]); if constexpr (G==16) vrcp(z[2]); if constexpr (G==18) vrcp(z[3]);
  if constexpr (G==15) vmul(p[0],p[0],z[0]); if constexpr (G==17) vmul(p[1],p[1],z[1]); if constexpr (G==21) vmul(p[2],p[2],z[2]); if constexpr (G==23) vmul(p[3],p[3],z[3]);
}
template<int TT,int HF,int G> DI void p1_cvt(P1& c){ float* p=c.tp[HF][TT];
  if constexpr (G==1) vcvtpk(c.pk[TT&1][2*HF],p[0],p[1]); if constexpr (G==3) vcvtpk(c.pk[TT&1][2*HF+1],p[2],p[3]); }
template<int TT> DI void p1_store(P1& c){ const unsigned* k=c.pk[TT&1]; asm volatile("buffer_store_dwordx4 %0, %1, %2, %3 offen sc1" :: "v"(u32x4{k[0],k[1],k[2],k[3]}), "v"(c.stoff), "s"(c.srdact), "s"(c.st_prev+TT*1024) : "memory"); }
template<int SL> DI void p2_issue(P2& d, int j){
  if(j==0) { set_m0<SL*P2_SLOT>(d.m0w); asm volatile("s_nop 0"); glds_off<0>(d.vw[0],d.srdw,d.sow); }
  if(j==1) glds_off<1024>(d.vw[1],d.srdw,d.sow);
  if(j==2) glds_off<2048>(d.vw[2],d.srdw,d.sow);
  if(j==3) { glds_off<3072>(d.vw[3],d.srdw,d.sow); set_m0<SL*P2_SLOT+4096>(d.m0w); }
  if(j==4) glds_half<0>(d.vw[4],d.srdw,d.sow);
  if(j==5) glds_off_sc1<512>(d.va,d.srda,d.soa);
  if(j==6) glds_off_sc1<1536>(d.va,d.srda,d.soa);
}
DI void p2_next(P2& d){ asm volatile("s_and_b32 %0, %3, 31\n\ts_lshr_b64 %2, %2, 5\n\ts_mul_i32 %1, %0, 0x9000\n\ts_lshl_b32 %0, %0, 13\n\ts_add_u32 %1, %1, %4\n\ts_add_u32 %0, %0, %5"
   : "=&s"(d.soa), "=&s"(d.sow), "+s"(d.tab) : "s"((unsigned)d.tab), "s"(d.sowb), "s"(d.soab) : "scc"); }
template<int ST,int PC> DI void p1_glds(P1& c){ constexpr int SL=(ST+5)%6; const unsigned so=(ST==0? c.so_cur : c.so_next) + (unsigned)((96*SL+8*PC)*9216-(PC&3)*1024); glds_off<(PC&3)*1024>(c.voff, c.srdw, so); }
template<int K,int TT> DI void p1_xload(P1& c){
  if constexpr (K<16) asm volatile("buffer_load_dwordx4 %0, %1, %2, %3 offen offset:%4 sc1" : "=a"(c.x[K][TT]) : "v"(c.vl), "s"(c.srdx), "s"((TT*18+(K&~3))*1024), "i"((K&3)*1024));
  else asm volatile("buffer_load_dwordx4 %0, %1, %2, %3 offen offset:%4 sc1" : "=v"(c.x[K][TT]) : "v"(c.vl), "s"(c.srdx), "s"((TT*18+(K&~3))*1024), "i"((K&3)*1024)); }
template<int ST,int TL,int KK,int M,int MODE> DI void p1_gap(P1& c, P2& d){
  constexpr bool FULL=MODE<2;
  constexpr int G=8*KK+M, SG=2*ST+TL, K=3*ST+KK;
  { const u32x4 a=cat(c.flo[KK][M>>2],c.fhi[KK][M>>2]); p1_mfma<(K==0? (MODE==0?2:1):0),(K<16)>(c.acc[TL][M>>2][M&3], a, c.x[K][M&3]); }
  if constexpr (M&1) {
    constexpr int N=(KK+2)%3; constexpr bool same=KK==0, toB= !same && TL==0 && FULL; constexpr int S2=(same||toB)? ST : (ST+1)%6;
    constexpr int IMM=(S2&1)*P1_SLOT+(M>>2)*P1_REG+N*4096+((M>>1)&1)*2048;
    const unsigned b=((same&&TL)||toB)? c.bB[S2>>1] : c.bA[S2>>1];
    if constexpr (FULL || ST<5 || same) { if constexpr ((M>>1)&1) trread<IMM>(c.fhi[N][M>>2], b); else trread<IMM>(c.flo[N][M>>2], b); }
  }
  if constexpr (FULL) {
    constexpr int PC=3*TL+KK;
    if constexpr (MODE==1 || ST>0) {
      if constexpr (M==4 && (PC==0||PC==4)) set_m0<((ST+5)%6)*P1_SLOT+PC*1024>(c.m0base);
      if constexpr (M==6) p1_glds<ST,PC>(c); }
    if constexpr (SG==11||(SG==0&&MODE==1)) p1_drs<(SG==0),dr_cum(G),dr_cum(G+1)>(c);
    if constexpr (MODE==1) {
      if constexpr (SG>=1 && SG<=8) p1_grp<((SG-1)>>1),((SG-1)&1),G>(c);
      if constexpr (SG>=2 && SG<=9) p1_cvt<((SG-2)>>1),((SG-2)&1),G>(c);
      if constexpr (G==20 && (SG==3||SG==5||SG==7||SG==9)) p1_store<(SG-3)/2>(c);
    } else if constexpr (MODE==0) {
      if constexpr (ST<4 && (M==0||M==2)) p1_xload<3*(ST+2)+(PC>>1),2*(PC&1)+(M>>1)>(c);
      if constexpr (G==20 && SG==9) asm volatile("buffer_load_dword %0, %0, %1, 0 offen" : "+v"(c.vwarm) : "s"(d.srdw) : "memory");
    }
  } else {
    if constexpr (ST==0) { p1_drs<1,dr_cum(G),dr_cum(G+1)>(c);
      if constexpr (M==1 && KK!=1) set_m0<5*P1_SLOT+KK*2048>(c.m0base);
      if constexpr (M==2) p1_glds<0,2*KK>(c); if constexpr (M==6) p1_glds<0,2*KK+1>(c); }
    if constexpr (ST>=1 && ST<=4) { p1_grp<ST-1,0,G>(c); p1_grp<ST-1,1,(G^1)>(c); }
    if constexpr (ST>=2) { p1_cvt<ST-2,0,G>(c); p1_cvt<ST-2,1,(G^1)>(c); }
    if constexpr (ST>=2) { constexpr int j= G==6?0: G==7?1: G==14?2: G==15?3: G==20?4: G==22?5: G==23?6: -1; if constexpr (j>=0) p2_issue<ST-2>(d,j);
      if constexpr (G==21) p1_store<ST-2>(c); if constexpr (G==23) p2_next(d); }
  }
}
template<int ST,int TL,int KK,int FULL> DI void p1_step(P1& c, P2& d){
  constexpr bool LAST=FULL==2 && ST==5;
  WAITL(LAST&&KK==2? 2:6);
  p1_gap<ST,TL,KK,0,FULL>(c,d); p1_gap<ST,TL,KK,1,FULL>(c,d); p1_gap<ST,TL,KK,2,FULL>(c,d); p1_gap<ST,TL,KK,3,FULL>(c,d);
  WAITL(LAST&&KK==2? 0: LAST&&KK==1? 4:6);
  p1_gap<ST,TL,KK,4,FULL>(c,d); p1_gap<ST,TL,KK,5,FULL>(c,d); p1_gap<ST,TL,KK,6,FULL>(c,d); p1_gap<ST,TL,KK,7,FULL>(c,d);
}
template<int ST,int TL,int FULL> DI void p1_section(P1& c, P2& d){ p1_step<ST,TL,0,FULL>(c,d); p1_step<ST,TL,1,FULL>(c,d); p1_step<ST,TL,2,FULL>(c,d); }
template<int ST,int MODE> DI void p1_stage(P1& c, P2& d, unsigned bA_next){
  if constexpr (MODE==2 && ST==2) flag_load(d.pv,d.pflag);
  if constexpr (MODE==1 && ST==2) flag_store(d.f1,d.vtag);
  p1_section<ST,0,MODE>(c,d);
  if constexpr (ST==5 && MODE==1) {
    #pragma unroll
    for(int k=0;k<3;k++) c.bA[k]=bA_next+k*2*P1_SLOT; }
  if constexpr (MODE<2) p1_section<ST,1,MODE>(c,d);
  if constexpr (MODE==0) WAITV(ST==0?12: ST<4?18: ST==4?7:19);
  if constexpr (MODE==1) WAITV(ST<2?19: ST==2?20: ST<5?21:20);
  if constexpr (MODE==2) { if constexpr (ST==0) WAITV(19); if constexpr (ST==1) WAITV(12); if constexpr (ST==2) { WAITV(8); flag_check(d.pv,d.pflag,d.tag); } if constexpr (ST==5) WAITV(16); }
  BAR();
}
template<int MODE> DI void p1_unit(P1& c, P2& d, unsigned bn){ p1_stage<0,MODE>(c,d,bn); p1_stage<1,MODE>(c,d,bn); p1_stage<2,MODE>(c,d,bn); p1_stage<3,MODE>(c,d,bn); p1_stage<4,MODE>(c,d,bn); p1_stage<5,MODE>(c,d,bn); }
template<int I> DI void p1_fin(P1& c){
  if constexpr (I<108) { constexpr int tt=I/27, k=I%27, r=k&3; f32x4 &g=c.acc[0][0][tt], &u=c.acc[0][1][tt]; float t;
    if constexpr (k<4) { vmul(t,g[r],u[r]); u[r]=t; }
    else if constexpr (k<8) { vmul(t,g[r],c.c1[tt]); g[r]=t; }
    else if constexpr (k<12) { t=g[r]; vexp(t); g[r]=t; }
    else if constexpr (k<16) { vfma(t,g[r],c.ir2[tt],c.ir2[tt]); g[r]=t; }
    else if constexpr (k<20) { t=g[r]; vrcp(t); g[r]=t; }
    else if constexpr (k<24) { vmul(t,u[r],g[r]); u[r]=t; }
    else if constexpr (k<26) vcvtpk(c.pk[tt&1][k-24],u[2*(k-24)],u[2*(k-24)+1]);
    else bstore64_sc1(u32x2{c.pk[tt&1][0],c.pk[tt&1][1]}, c.stoff, c.srdact, c.st_prev+tt*1024); }
}
template<bool Z> DI void p2_mfma(f32x4& acc, const u32x4& a, const u32x4& b){
  if constexpr (Z) asm volatile("v_mfma_f32_16x16x32_bf16 %0, %1, %2, 0" : "=&v"(acc) : "v"(a), "a"(b));
  else asm volatile("v_mfma_f32_16x16x32_bf16 %0, %1, %2, %0" : "+v"(acc) : "v"(a), "a"(b)); }
template<bool E,int I> DI void p2_fin(P1& e){ if constexpr (E) { p1_fin<3*I>(e); p1_fin<3*I+1>(e); p1_fin<3*I+2>(e); } }
template<int P,int J,bool Z> DI void p2_step(P2& c, P1& e){
  constexpr int PAR=P&1, ST=PAR*9+J, CUR=ST%6, NX=(ST+3)%6; constexpr int JN=(J+3)%9; constexpr int PN=(J+3<9)?P:(P+1)%6;
  constexpr int N = J<3?4: J==3?5: J==4?6: J<7?7: J==7?6:5;
  constexpr int IMM=(PN&1)*P2_SLOT+(JN<8?(JN>>2)*1024:0); constexpr int BI=JN<8?(JN&3):4;
  constexpr int P1n=(P+1)%6, PF=(P+5)%6;
  WAITL(N);
  u32x4 a=cat(c.flo[CUR],c.fhi[CUR]);
  p2_mfma<Z>(c.acc[J][0],a,c.bf[PAR][0]);
  trread<IMM>(c.flo[NX], c.ba[PN>>1][BI]);
  p2_fin<Z,4*J>(e);
  p2_mfma<Z>(c.acc[J][1],a,c.bf[PAR][1]);
  trread<IMM+2*P2_WREG>(c.fhi[NX], c.ba[PN>>1][BI]);
  p2_fin<Z,4*J+1>(e);
  p2_mfma<Z>(c.acc[J][2],a,c.bf[PAR][2]);
  if constexpr (J>=2 && J<=5) ldsread128a<(P1n&1)*P2_SLOT+((J-2)>>1)*P2_WREG+((J-2)&1)*1024>(c.bf[PAR^1][J-2], c.bb[P1n>>1]);
  if constexpr (J==0) set_m0<PF*P2_SLOT>(c.m0w);
  if constexpr (J==7) asm volatile("s_bfe_u32 %0, %1, %2" : "=s"(c.kk) : "s"((unsigned)c.tab), "i"(5<<16) : "scc");
  if constexpr (J==8) asm volatile("s_mul_i32 %0, %1, 0x9000" : "=s"(c.sow) : "s"(c.kk));
  p2_fin<Z,4*J+2>(e);
  p2_mfma<Z>(c.acc[J][3],a,c.bf[PAR][3]);
  if constexpr (J<=3) glds_off<J*1024>(c.vw[J], c.srdw, c.sow);
  if constexpr (J==3) set_m0<PF*P2_SLOT+4096>(c.m0w);
  if constexpr (J==4) glds_half<0>(c.vw[4], c.srdw, c.sow);
  if constexpr (J==5) glds_off_sc1<512>(c.va, c.srda, c.soa);
  if constexpr (J==6) glds_off_sc1<1536>(c.va, c.srda, c.soa);
  if constexpr (J==7) { asm volatile("s_lshl_b32 %0, %1, 13\n\ts_lshr_b64 %2, %2, 5" : "=&s"(c.soa), "+s"(c.kk), "+s"(c.tab) :: "scc"); }
  if constexpr (J==8) { asm volatile("s_add_u32 %0, %0, %2\n\ts_add_u32 %1, %1, %3" : "+s"(c.sow), "+s"(c.soa) : "s"(c.sowb), "s"(c.soab) : "scc"); }
  p2_fin<Z,4*J+3>(e);
}
template<int P,bool Z> DI void p2_stage(P2& c, P1& e){
  if constexpr (P==3) flag_store(c.myflag+1,c.vtag);
  if constexpr (P==1) flag_load(c.pv,c.f2);
  p2_step<P,0,Z>(c,e); p2_step<P,1,Z>(c,e); p2_step<P,2,Z>(c,e); p2_step<P,3,Z>(c,e); p2_step<P,4,Z>(c,e); p2_step<P,5,Z>(c,e); p2_step<P,6,Z>(c,e); p2_step<P,7,Z>(c,e); p2_step<P,8,Z>(c,e);
  if constexpr (Z) WAITV(26); else if constexpr (P==2) WAITV(14); else if constexpr (P==1) { WAITV(7); flag_check(c.pv,c.f2,c.tag); } else WAITV(21);
  BAR();
}
template<int J,int U,int K> DI void p2_dump(P2& c){
  if constexpr (K<4) vcvtpk(c.r[K],c.acc[J][2*U+(K>>1)][2*(K&1)],c.acc[J][2*U+(K>>1)][2*(K&1)+1]);
  else asm volatile("buffer_store_dwordx4 %0, %1, %2, %3 offen sc1\n\tds_write_b128 %4, %0 offset:%5\n\ts_nop 0"
                    :: "v"(u32x4{c.r[0],c.r[1],c.r[2],c.r[3]}), "v"(c.vl), "s"(c.srdp), "s"(c.pb[U]+J*1024), "v"(c.ownw[U]), "i"(J*1024) : "memory");
}
template<int GI,int I> DI void p2_dumpq(P2& c){ if constexpr (GI>=0 && I>=0 && I<30) p2_dump<3*GI+I/10,(I/5)&1,I%5>(c); }
template<int GI,int S,int JJ> DI void p2_tstep(P2& c){
  constexpr int n=(GI*5+S)*3+JJ, CUR=(n+3)%6, NX=n%6, J=3*GI+JJ;
  constexpr int SN=S<4? S+1:0, JN=S<4? J : J+3, PN=SN+1;
  constexpr int IMM=(PN&1)*P2_SLOT+(JN<8?(JN>>2)*1024:0); constexpr int BI=JN<8?(JN&3):4;
  constexpr bool RD=n+3<45;
  if constexpr (GI==0) { if constexpr (JJ==0) WAITL(S==0?4:2); if constexpr (JJ==1) WAITL(S==0?6:S<4?8:4); if constexpr (JJ==2) WAITL(S<4?8:4); }
  else WAITL(n<43? 4 : n==43? 2:0);
  const u32x4 a=cat(c.flo[CUR],c.fhi[CUR]);
  const u32x4* b= S==0? c.bf[1] : c.bt[S-1<0?0:S-1];
  p2_mfma<false>(c.acc[J][0],a,b[0]);
  if constexpr (RD) trread<IMM>(c.flo[NX], c.ba[PN>>1][BI]);
  p2_mfma<false>(c.acc[J][1],a,b[1]);
  if constexpr (RD) trread<IMM+2*P2_WREG>(c.fhi[NX], c.ba[PN>>1][BI]);
  p2_mfma<false>(c.acc[J][2],a,b[2]);
  if constexpr (GI==0 && S<4 && JJ<2) ldsread128a<((S+2)&1)*P2_SLOT+JJ*P2_WREG>(c.bt[S][2*JJ], c.bb[(S+2)>>1]);
  p2_dumpq<GI-1,(S*3+JJ-1)*3>(c); p2_dumpq<GI-1,(S*3+JJ-1)*3+1>(c);
  p2_mfma<false>(c.acc[J][3],a,b[3]);
  if constexpr (GI==0 && S<4 && JJ<2) ldsread128a<((S+2)&1)*P2_SLOT+JJ*P2_WREG+1024>(c.bt[S][2*JJ+1], c.bb[(S+2)>>1]);
  p2_dumpq<GI-1,(S*3+JJ-1)*3+2>(c);
}
template<int GI,int S> DI void p2_tstage(P2& c){ p2_tstep<GI,S,0>(c); p2_tstep<GI,S,1>(c); p2_tstep<GI,S,2>(c);
  if constexpr (GI==0 && S<3) { WAITV(14-7*S); BAR(); } }
template<int GI> DI void p2_tgroup(P2& c){ p2_tstage<GI,0>(c); p2_tstage<GI,1>(c); p2_tstage<GI,2>(c); p2_tstage<GI,3>(c); p2_tstage<GI,4>(c); }
template<int I> DI void p2_dump_all(P2& c){ if constexpr (I<30){ p2_dumpq<2,I>(c); p2_dump_all<I+1>(c); } }
DI float row_bcast2(float v){
  auto a=__builtin_amdgcn_permlane32_swap(__float_as_uint(v),__float_as_uint(v),false,false);
  auto b=__builtin_amdgcn_permlane16_swap(a[1],a[1],false,false); return __uint_as_float(b[0]); }
DI float rows_sum(float v){ auto b=__builtin_amdgcn_permlane16_swap(__float_as_uint(v),__float_as_uint(v),false,false); return xhalf_sum(__uint_as_float(b[0])+__uint_as_float(b[1])); }
template<bool LO> DI unsigned slab_voff(int pc, int rr, int cc){
  const int R=6*pc+rr, S=R>>5, g=(R>>2)&3, j=4*((R>>4)&1)+(R&3), a=2*S+(g>>1), hp=g&1;
  const bool lo= LO || a<32; const unsigned row=mad24(18,lo? a : 8*(a-32)+4*hp+(j>>1),lo? 8*hp+j : 16+(j&1));
  return mad24(row,1152,cc*16); }
DI void slab_glds(unsigned m0, unsigned voff, u32x4 srd, unsigned soff){
  asm volatile("s_mov_b32 m0, %0\n\ts_mov_b64 exec, %4\n\tbuffer_load_dwordx4 %1, %2, %3 offen lds\n\ts_mov_b64 exec, -1" :: "s"(m0), "v"(voff), "s"(srd), "s"(soff), "s"(0x07FDFF7FDFF7FDFFull) : "memory"); }

__global__ __launch_bounds__(256,1) void k3(const u16* __restrict__ x, const u16* __restrict__ wqkv, const u16* __restrict__ wphi, const u16* __restrict__ wo, const u16* __restrict__ wg, const u16* __restrict__ wu, const u16* __restrict__ wd,
                                            char* __restrict__ ws, u16* __restrict__ out STAMP_ARG){
  STAMP_DECL STAMPIT(0);
  extern __shared__ __attribute__((aligned(16))) char lds[];
  asm volatile("" :: "s"(x), "s"(wqkv), "s"(wphi), "s"(wo), "s"(wg), "s"(wu), "s"(wd), "s"(ws), "s"(out));
  const int tid=threadIdx.x, lane=tid&63; const int w=rfl(tid>>6);
  const int xq=blockIdx.x&7, i=blockIdx.x>>3, isB=i&1;
  const int xcd=(xq&(8-MAP))|((i>>1)&(MAP-1)), T=(xq&(MAP-1))*(16/MAP)+(i>>1)/MAP;
  const int tb=(xq>>1)*8+(i&7), cg=(xq&1)*4+(i>>3);
  const int me=(T*2+isB)*8+xcd;
  const unsigned ldsb=(unsigned)(size_t)lds;
  GAS unsigned* flags=(GAS unsigned*)(ws+WS_FLAGS);
  const u32x4 srdws=make_srd(ws, WS_SMALL);
  const int g=lane>>4, l15=lane&15, q=l15>>2, p=lane&3;
  P1 c; P2 d;
  unsigned selL=0x3f80u, selH=0x3f800000u; asm volatile("" : "+s"(selL), "+s"(selH));
  const int th=w>>1, fh=w&1;
  c.srdw=make_srd(th? wu:wg, D_*F_*2);
  { const int cc=lane&7; c.voff=(lane>>3)*9216+(((cc>>1)^((lane>>4)&3))*32)+(cc&1)*16; }
  c.m0base=ldsb+th*P1_REG+fh*6*1024;
  const unsigned rowoff=(unsigned)(fh*48)*9216u;
  auto unit_of=[&](int it){ return it<4 ? (isB?5:0)+((it+T)&3) : 4; };
  auto so_of=[&](int it){ return (unsigned)((xcd*9+unit_of(it))*128)+rowoff; };
  #define WST(lo,hi) if(w==1||w==2){ _Pragma("unroll 1") for(int s=lo;s<hi;s++) _Pragma("unroll") for(int j=0;j<12;j++) glds_m0(ldsb+th*P1_REG+s*P1_SLOT+j*1024, c.voff, c.srdw, so_of(0)-rowoff+(unsigned)(96*s+8*j)*9216u); }
  unsigned tag, vtag;
  const int h=lane>>5, blk=(lane>>4)&1; const unsigned vl=lane*16;
  const u32x4 srdo=make_srd(wo, D_*D_*2);
  unsigned sv[8]; u32x2 xr[5]; float mx;
  #define SLAB(lo,hi) _Pragma("unroll") for(int it=lo;it<hi;it++) slab_glds(ldsb+FR_SLAB+(w+4*it)*960, sv[it<20? it&3 : it-16], srdo, cg*144+(it<20?(it>>2)*108*1152:0));
  {
    const int tbA=tb, hg=cg, head=4*hg+w, off=2*(w&1);
    const u32x4 srdx=make_srd(x+(size_t)tbA*64*D_, 64*D_*2), srdw=make_srd(wqkv, D_*3*D_*2);
    const unsigned vx0=(lane>>3)*1152+(((lane&7)^(lane>>4))*16), vx1=(vx0^64)+8*1152;
    #pragma unroll
    for(int b=0;b<9;b++){ glds_m0(ldsb+w*2048+b*8192, vx0, srdx, w*16*1152+b*128); glds_m0(ldsb+w*2048+b*8192+1024, vx1, srdx, w*16*1152+b*128); }
    flag_load(vtag, flags+64*128+64);
    unsigned tw=0;
    { const int k=lane&3, b=(lane>>2)&7; const char* p= b==0? (const char*)wg : b==1? (const char*)wu : b==2? (const char*)wd : b==3? (const char*)out : (const char*)wo;
      const unsigned len= b<3? D_*F_*2 : b==3? S_*D_*2 : D_*D_*2; unsigned o=k<<21; o= o<len? o : len-4;
      if(lane>=32){ p=ws; o=(lane-32)<<21; o= o<WS_END? o : WS_END-4; }
      asm volatile("global_load_dword %0, %1, off" : "=v"(tw) : "v"(p+o) : "memory"); }
    unsigned cw;
    { u64 pc; asm volatile("s_getpc_b64 %0" : "=s"(pc)); const u32x4 srdc=make_srd((const void*)(pc&~127ull), CODE_BYTES);
      bload32<0,false>(cw, (lane<13 && w==3)? lane*128 : 0x80000000u, srdc, i*13*128); }
    u32x4 wr0, wr1; const unsigned worw = w*256+blockIdx.x;
    bload128<0,false>(wr0, vl, srdo, worw*1152); bload128<1024,false>(wr1, lane<8? vl:0x80000000u, srdo, worw*1152);
    asm volatile("s_mov_b32 m0, %0\n\ts_nop 0\n\tbuffer_load_dwordx4 %1, %3, 0 offen lds\n\tbuffer_load_dword %2, %3, 0 offen offset:640 lds"
                 :: "s"(ldsb+K1_WPHI+3712+w*1024), "v"(lane<40? vl:0x80000000u), "v"(lane*4), "s"(make_srd(wphi+head*324, 648)) : "memory");
    unsigned wq[2];
    { const unsigned r15=divm<69,10>(lane), c15=lane-r15*15; const unsigned v=mad24(r15,3456,mad24(divm<52,8>(c15),512,c15*128))|(lane<WQW? 0:0x80000000u);
      bload32<0,false>(wq[0], v, srdw, (i*18+w*4)*3456+(xq&1)*512); bload32<0,false>(wq[1], (w==0 && lane<30)? v:0x80000000u, srdw, (i*18+16)*3456+(xq&1)*512); }
    unsigned vw[7];
    #pragma unroll
    for(int k=0;k<7;k++){ const unsigned ck=(w+4*k)*64+lane, row=divm<2428,16>(ck), cc=ck-__umul24(row,27); vw[k]=mad24(row,3456,mad24(divm<57,9>(cc),1008,cc*16)); }
    vw[6]|= (unsigned)(w+1)<<29&0x80000000u;
    const unsigned sw0=hg*144, mw0=ldsb+K1_RING+w*1024;
    #define A_W(PR,LO,HI) _Pragma("unroll") for(int k=LO;k<HI;k++) glds_m0(mw0+(PR)*K1_PAIR+k*4096, vw[k], srdw, sw0+(PR)*64*3456);
    STAMPIT(28);
    A_W(0,0,7)
    unsigned bx[4], bw[2];
    { const int row=lane&31;
      #pragma unroll
      for(int e=0;e<4;e++) bx[e]=ldsb+row*128+(((2*e+h)^((row>>1)&7))*16);
      const int hoff=36*w-2*off;
      #pragma unroll
      for(int t=0;t<2;t++){ unsigned gi=8*t+4*blk+p; gi=gi>14?14:gi; bw[t]=ldsb+K1_RING+mad24(8*h+q,432,mad24(divm<52,8>(gi),104,8*gi))+hoff; } }
    unsigned warm[2], wdst[2];
    { const unsigned r9=divm<57,9>(lane); warm[0]=mad24(r9,9216-1152,lane*128)|(lane<54? 0:0x80000000u); warm[1]= fh? 0x80000000u : warm[0]+6*9216; }
    f32x16 acc[2][2]={}; f32x4 cs4={}; float s1[2]={0,0}, s2[2]={0,0};
    u32x4 sel[2];
    #pragma unroll
    for(int t=0;t<2;t++){ const unsigned v= l15==2*t+(g&1)? 0x3f803f80u:0u; sel[t]=u32x4{v,v,v,v}; }
    unsigned wa[8];
    #pragma unroll
    for(int k=0;k<4;k++){ const unsigned idx=lane+64*k, dd=divm<683,13>(idx), fr=(idx-dd*12)*2, reg=(fr&3)+4*(fr>>3); const unsigned o=mad24(dd,36,((fr>>2)&1)*18+reg*2);
      wa[2*k]=ldsb+K1_WPHI+3712+w*1024+((reg<9 && dd<18)? o:648); wa[2*k+1]=ldsb+K1_WPHI+3712+w*1024+((reg+1<9 && dd<18)? o+2:648); }
    asm volatile("" : "+v"(acc[0][0]), "+v"(acc[0][1]), "+v"(acc[1][0]), "+v"(acc[1][1]), "+v"(cs4), "+v"(sel[0]), "+v"(sel[1]), "+v"(bx[0]), "+v"(bx[1]), "+v"(bx[2]), "+v"(bx[3]), "+v"(bw[0]), "+v"(bw[1]), "+v"(warm[0]), "+v"(warm[1]));
    asm volatile("" : "+v"(wa[0]), "+v"(wa[1]), "+v"(wa[2]), "+v"(wa[3]), "+v"(wa[4]), "+v"(wa[5]), "+v"(wa[6]), "+v"(wa[7]));
    A_W(1,0,7)
    asm volatile("s_waitcnt vmcnt(14)" : "+v"(vtag), "+v"(wr0), "+v"(wr1), "+v"(cw), "+v"(tw), "+v"(wq[0]), "+v"(wq[1]) :: "memory");
    {
      const unsigned a=ldsb+K1_WPHI+w*912+lane*4; unsigned v[4];
      #pragma unroll
      for(int k=0;k<4;k++) v[k]=*LDSP(u16,wa[2*k])|((unsigned)*LDSP(u16,wa[2*k+1])<<16);
      *LDSP(unsigned,a)=v[0]; *LDSP(unsigned,a+256)=v[1]; *LDSP(unsigned,a+512)=v[2]; if(lane<36) *LDSP(unsigned,a+768)=v[3]; }
    tag=rfl(vtag)*0x9E3779B1u+0x7F4A7C15u; vtag=tag; asm volatile("" : "+v"(vtag));
    { float s=0.f;
      #pragma unroll
      for(int k=0;k<4;k++){ s=dot2(wr0[k],0x3f803f80u,s); s=dot2(wr1[k],0x3f803f80u,s); }
      s=dpp_add<0xB1>(s); s=dpp_add<0x4E>(s); s=dpp_add<0x141>(s); s=dpp_add<0x140>(s); s=rows_sum(s);
      const unsigned hi=pk2(s,0.f)&0xffffu; const unsigned lo=pk2(s-lo2f(hi),0.f);
      const unsigned hd=(worw*3641u)>>16, dd=worw-hd*18;
      const unsigned a= dd<16? hd : 32+(hd>>3), hp= dd<16? dd>>3 : (hd>>2)&1, j= dd<16? dd&7 : 2*(hd&3)+(dd-16);
      bstore64_sc1(u32x2{hi|(lo<<16),vtag}, (lane || worw>=576)? 0x80000000u:0u, srdws, WS_WOROW+(32*(a>>1)+16*(j>>2)+4*(2*(a&1)+hp)+(j&3))*8); }
    u32x4 wq0,wq1,wk0,wk1;
    { const unsigned wb=ldsb+K1_WPHI+w*912+blk*32+p*8;
      auto wrow=[&](int dd){ return wb+((unsigned)dd<18u? dd:18)*48; };
      const int R=4*h+q;
      wk0=trfrag(wrow(R>=4? R-4-off:99),wrow(R+4-off)); wk1=trfrag(wrow(R+12-off),wrow(99));
      wq0=trfrag(wrow(R-off),wrow(R+8-off)); wq1=trfrag(wrow(R<4? R+16-off:99),wrow(99)); }
    A_W(2,0,3)
    asm volatile("s_waitcnt vmcnt(11) lgkmcnt(0)\n\ts_barrier" : "+v"(wq0), "+v"(wq1), "+v"(wk0), "+v"(wk1) :: "memory");
    STAMPIT(22);
    u32x4 xf[2][2][2], st[2]; u32x2 al[2][4], ah[2][4];
    const unsigned bst=ldsb+w*2048+vl;
    #define K1_X(B,E,TT,XB,E2) xf[B][E][TT]=*LDSP(u32x4,bx[2*(E2)+(E)]+(XB)*8192+(TT)*4096);
    #define K1_A(B,I,SL) { al[B][I]=trb(bw[(I)&1]+k1_slot(SL)+((I)>>1)*6912); ah[B][I]=trb(bw[(I)&1]+k1_slot(SL)+((I)>>1)*6912+1728); }
    K1_X(0,0,0,0,0) K1_X(0,0,1,0,0) K1_A(0,0,0) K1_A(0,1,0) K1_X(0,1,0,0,0) K1_X(0,1,1,0,0) K1_A(0,2,0) K1_A(0,3,0) st[0]=*LDSP(u32x4,bst);
    #define MM(D,I,B) SB(); mv32<false>(D,cat(al[cu][I],ah[cu][I]),B); SB();
    #define MC(D,I) SB(); mv16(cs4,sel[(I)&1],cat(al[cu][I],ah[cu][I])); SB();
    #define WL(K) if(cu==0 || (K)<3) glds_m0(mw0+(((j+5)%6)>>1)*K1_PAIR+((cu?0:3)+(K))*4096, vw[(cu?0:3)+(K)], srdw, sw);
    #define SD(K) s1[cu]=dot2(st[cu][K],0x3f803f80u,s1[cu]); s2[cu]=dot2(st[cu][K],st[cu][K],s2[cu]);
    #pragma unroll 1
    for(int s6=0;s6<18;s6+=6){
      #pragma unroll
      for(int j=0;j<6;j++){
        const int s=s6+j; const int cu=j&1, n=cu^1;
        const unsigned sw=sw0+((s+5)>>1)*64*3456;

        MM(acc[0][0],0,xf[cu][0][0])
        WL(0)
        MM(acc[0][1],0,xf[cu][0][1])
        K1_X(n,0,0,(s+1)>>1,n) K1_X(n,0,1,(s+1)>>1,n) SD(0)
        MC(cs[0],0)
        K1_A(n,0,(j+1)%6) SD(1)
        MM(acc[1][0],1,xf[cu][0][0])
        WL(1)
        MM(acc[1][1],1,xf[cu][0][1])
        K1_A(n,1,(j+1)%6) SD(2)
        MC(cs[1],1)
        K1_X(n,1,0,(s+1)>>1,n) K1_X(n,1,1,(s+1)>>1,n) SD(3)
        MM(acc[0][0],2,xf[cu][1][0])
        WL(2)
        MM(acc[0][1],2,xf[cu][1][1])
        K1_A(n,2,(j+1)%6)
        MC(cs[0],2)
        K1_A(n,3,(j+1)%6)
        MM(acc[1][0],3,xf[cu][1][0])
        WL(3)
        MC(cs[1],3)
        st[n]=*LDSP(u32x4,bst+((s+1)>>1)*8192+n*1024);
        MM(acc[1][1],3,xf[cu][1][1])
        asm volatile("s_waitcnt vmcnt(%0)\n\ts_barrier" :: "i"(cu? 10:7) : "memory");
      }
    }
    #undef MM
    #undef SD
    #undef K1_X
    #undef K1_A
    #undef WL
    #undef A_W
    STAMPIT(23);
    { const int row=w*16+l15;
      #pragma unroll
      for(int j=0;j<5;j++){ const unsigned cb=cg*144+32*j+8*(j<4? g:g&1); xr[j]=*LDSP(u32x2,ldsb+(cb>>7)*8192+row*128+((((cb>>4)&7)^((row>>1)&7))*16)+(cb&8)); } }
    asm volatile("s_waitcnt lgkmcnt(0)\n\ts_barrier" : "+v"(xr[0]), "+v"(xr[1]), "+v"(xr[2]), "+v"(xr[3]), "+v"(xr[4]) :: "memory");
    asm volatile("s_nop 7" : "+v"(cs4));
    if(lane<16){
      #pragma unroll
      for(int k=0;k<4;k++) *LDSP(float,ldsb+K1_STAT+512+w*256+k*64+lane*4)=cs4[k]; }
    #pragma unroll
    for(int j=0;j<2;j++){ float a=s1[j], b=s2[j];
      a=dpp_add<0xB1>(a); b=dpp_add<0xB1>(b); a=dpp_add<0x4E>(a); b=dpp_add<0x4E>(b); a=dpp_add<0x141>(a); b=dpp_add<0x141>(b);
      const float m=a*(1.f/D_), r=__builtin_amdgcn_rsqf(fmaxf(b*(1.f/D_)-m*m,0.f)+1e-5f);
      *LDSP(f32x2,ldsb+K1_STAT+(w*16+j*8+(lane>>3))*8)=f32x2{-m,r}; }
    asm volatile("s_waitcnt vmcnt(0) lgkmcnt(0)\n\ts_barrier" ::: "memory");
    bload32<0,false>(wdst[0], warm[0], c.srdw, xcd*1152+((T*2+isB)*18+fh*12)*9216+NOFW); bload32<0,false>(wdst[1], warm[1], c.srdw, xcd*1152+((T*2+isB)*18+fh*12)*9216+NOFW);
    asm volatile("" : "+v"(acc[0][0]), "+v"(acc[0][1]), "+v"(acc[1][0]), "+v"(acc[1][1]));
    f32x16 cs[2];
    #pragma unroll
    for(int t=0;t<2;t++)
      #pragma unroll
      for(int k=0;k<4;k++){ const f32x4 v=*LDSP(f32x4,ldsb+K1_STAT+512+w*256+t*128+k*32+h*16); cs[t][4*k]=v[0]; cs[t][4*k+1]=v[1]; cs[t][4*k+2]=v[2]; cs[t][4*k+3]=v[3]; }
    const f32x2 mr[2]={*LDSP(f32x2,ldsb+K1_STAT+(lane&31)*8), *LDSP(f32x2,ldsb+K1_STAT+256+(lane&31)*8)};
    mx=-*LDSP(float,ldsb+K1_STAT+(w*16+l15)*8);
    const unsigned PK=ldsb+w*10240, VV=PK+5120+64, KT=VV+3072;
    u32x4 f00[2], f01[2], f10[2]; f32x16 pk[2], pq[2];
    #pragma unroll
    for(int t=0;t<2;t++){
      acc[0][t]=__builtin_elementwise_fma(cs[0],(f32x16)mr[t][0],acc[0][t]); acc[1][t]=__builtin_elementwise_fma(cs[1],(f32x16)mr[t][0],acc[1][t]);
      f00[t]=packfrag(acc[0][t],0); f01[t]=packfrag(acc[0][t],1); f10[t]=packfrag(acc[1][t],0); }
    mz32(pk[0],wk0,f01[0]); mz32(pk[1],wk0,f01[1]); mv32<true>(pk[0],wk1,f10[0]); mv32<true>(pk[1],wk1,f10[1]);
    mz32(pq[0],wq0,f00[0]); mz32(pq[1],wq0,f00[1]); mv32<false>(pq[0],wq1,f01[0]); mv32<false>(pq[1],wq1,f01[1]);
    asm volatile("s_nop 7" : "+v"(pk[0]), "+v"(pk[1]));
    #pragma unroll
    for(int t=0;t<2;t++){
      unsigned ok[9]; hedgehog(pk[t],mr[t][1],ok);
      const int tok=32*t+(lane&31); const float r=mr[t][1]; const f32x16& v=acc[1][t];
      const unsigned pa=PK+tok*80+h*40;
      *LDSP(u32x2,pa)=u32x2{ok[0],ok[1]}; *LDSP(u32x2,pa+8)=u32x2{ok[2],ok[3]}; *LDSP(u32x2,pa+16)=u32x2{ok[4],ok[5]}; *LDSP(u32x2,pa+24)=u32x2{ok[6],ok[7]}; *LDSP(u32x2,pa+32)=u32x2{ok[8],0u};
      const unsigned va=VV+tok*48+h*8;
      *LDSP(u32x2,va)=u32x2{pk2(v[4]*r,v[5]*r),pk2(v[6]*r,v[7]*r)}; *LDSP(u32x2,va+16)=u32x2{pk2(v[8]*r,v[9]*r),pk2(v[10]*r,v[11]*r)};
      *LDSP(u32x2,va+32)= h? u32x2{0x3f803f80u,0x3f803f80u} : u32x2{pk2(v[12]*r,v[13]*r),pk2(v[14]*r,v[15]*r)}; }
    { f32x16 kv0, kv1;
      const unsigned pa=PK+(8*h+q)*80+blk*32+p*8, va=VV+(8*h+q)*48+blk*32+p*8;
      #pragma unroll
      for(int s=0;s<4;s++){ const u32x4 b=trfrag(va+s*768,va+s*768+192), a0=trfrag(pa+s*1280,pa+s*1280+320), a1=trfrag(pa+64+s*1280,pa+64+s*1280+320);
        if(s==0){ mz32(kv0,a0,b); mz32(kv1,a1,b); } else { mv32<false>(kv0,a0,b); mv32<false>(kv1,a1,b); } }
      MPAD(kv0,kv1);
      const int vc=lane&31, r19 = vc==20 ? 18 : vc-off; const unsigned dst=KT+r19*80+8*h;
      if((unsigned)(vc-off)<18u || vc==20){ *LDSP(u32x2,dst)=u32x2{pk2(kv0[0],kv0[1]),pk2(kv0[2],kv0[3])}; *LDSP(u32x2,dst+16)=u32x2{pk2(kv0[4],kv0[5]),pk2(kv0[6],kv0[7])};
        *LDSP(u32x2,dst+32)=u32x2{pk2(kv0[8],kv0[9]),pk2(kv0[10],kv0[11])}; *LDSP(u32x2,dst+48)=u32x2{pk2(kv0[12],kv0[13]),pk2(kv0[14],kv0[15])}; *LDSP(u32x2,dst+64)=u32x2{pk2(kv1[0],kv1[1]),pk2(kv1[2],kv1[3])}; }
      const u32x4 t0=*LDSP(u32x4,KT+vl), t1=*LDSP(u32x4,KT+1024+(vl&511));
      const unsigned so=WS_KVP+(head*32+tbA)*1536;
      bstore128_sc1(t0, vl, srdws, so); bstore128_sc1(t1, h? 0x80000000u:vl, srdws, so+1024); }
    STAMPIT(24);
    asm volatile("" : "+v"(pq[0]), "+v"(pq[1]));
    unsigned oq[2][9];
    #pragma unroll
    for(int t=0;t<2;t++) hedgehog(pq[t],mr[t][1],oq[t]);
    asm volatile("s_waitcnt vmcnt(0)" : "+v"(oq[0][8]), "+v"(oq[1][8]), "+v"(wdst[0]), "+v"(wdst[1])); flag_store(flags+(32+head)*128+64+tbA, vtag);
    #pragma unroll
    for(int t=0;t<2;t++){ const unsigned so=WS_FQ+(head*64+tbA*2+t)*2304;
      bstore128_sc1(u32x4{oq[t][0],oq[t][1],oq[t][2],oq[t][3]}, vl, srdws, so); bstore128_sc1(u32x4{oq[t][4],oq[t][5],oq[t][6],oq[t][7]}, vl, srdws, so+1024); bstore32_sc1(oq[t][8], lane*4, srdws, so+2048); }
    STAMPIT(25);
    WAITV(0);
    flag_store(flags+(32+head)*128+96+tbA, vtag);
    STAMPIT(26);
  }
  {
    const int head=(i&3)*8+xq, tr=i>>2;
    poll_all(flags+(32+head)*128+64, (lane&31)*4, w, tag);
    BAR();
    STAMPIT(27);
    u32x4 pv[16], q0[2], q1[2]; unsigned q2[2]; u32x2 wr[3];
    { const unsigned vp= (lane&31)<24? h*1536+(lane&31)*16 : 0x80000000u;
      #pragma unroll
      for(int k=0;k<8;k++){ bload128<0,true>(pv[2*k], vp, srdws, WS_KVP+head*49152+w*384+k*6144); bload128<3072,true>(pv[2*k+1], vp, srdws, WS_KVP+head*49152+w*384+k*6144); } }
    bload64<0,true>(wr[0],tid*8,srdws,WS_WOROW); bload64<2048,true>(wr[1],tid*8,srdws,WS_WOROW); bload64<0,true>(wr[2],(tid&63)*8,srdws,WS_WOROW+4096);
    { const int rr10=divm<103,10>(lane), cc10=lane-rr10*10;
      #pragma unroll
      for(int k=0;k<4;k++){ sv[k]=slab_voff<true>(w+4*k,rr10,cc10); sv[4+k]=slab_voff<false>(w+4*(20+k),rr10,cc10); asm volatile("" : "+v"(sv[k]), "+v"(sv[4+k])); } }
    { const int r=lane&31; const unsigned one= r<8? ((r&1)? 0x3f800000u:0x3f80u) : 0u;
      const u32x4 sel={(r>>1)==0?one:0u,(r>>1)==1?one:0u,(r>>1)==2?one:0u,(r>>1)==3?one:0u};
      f32x16 z;
      #pragma unroll
      for(int k=0;k<16;k++){ asm volatile("s_waitcnt vmcnt(%1)" : "+v"(pv[k]) : "i"(18-k)); if(k==0) mz32(z,sel,pv[0]); else mv32<false>(z,sel,pv[k]); }
      asm volatile("s_nop 7\n\ts_nop 3" : "+v"(z));
      const bool zero= w==3 && r>=23;
      if(r<24 || (w==3 && r<28)) *LDSP(u32x2,ldsb+FR_KVB+(24*w+r)*16+8*h)= zero? u32x2{0,0} : u32x2{pk2(z[0],z[1]),pk2(z[2],z[3])}; }
    STAMPIT(1);
    for(int n=0;n<0x100000;n++){
      asm volatile("s_waitcnt vmcnt(0)" : "+v"(wr[0]), "+v"(wr[1]), "+v"(wr[2]));
      if(__builtin_amdgcn_ballot_w64(wr[0][1]!=tag || wr[1][1]!=tag || wr[2][1]!=tag)==0) break;
      __builtin_amdgcn_s_sleep(2);
      bload64<0,true>(wr[0],tid*8,srdws,WS_WOROW); bload64<2048,true>(wr[1],tid*8,srdws,WS_WOROW); bload64<0,true>(wr[2],(tid&63)*8,srdws,WS_WOROW+4096); }
    asm volatile("s_waitcnt vmcnt(0)" : "+v"(wr[0]), "+v"(wr[1]), "+v"(wr[2]));
    { const unsigned a=ldsb+FR_SLAB+tid*160+144; *LDSP(u32x4,a)=u32x4{wr[0][0],0,0,0}; *LDSP(u32x4,a+256*160)=u32x4{wr[1][0],0,0,0}; if(tid<64) *LDSP(u32x4,a+512*160)=u32x4{wr[2][0],0,0,0}; }
    poll_all(flags+(32+head)*128+96+tr*4+w, 0, 0, tag);
    #pragma unroll
    for(int t=0;t<2;t++){ const unsigned so=WS_FQ+(head*64+tr*8+w*2+t)*2304; bload128<0,true>(q0[t],vl,srdws,so); bload128<1024,true>(q1[t],vl,srdws,so); bload32<2048,true>(q2[t],lane*4,srdws,so); }
    SLAB(0,SLA)
    asm volatile("s_waitcnt lgkmcnt(0)\n\ts_barrier" ::: "memory");
    STAMPIT(16);
    const int r=lane&31, hf=(r>>2)&1, reg=(r&3)+4*(r>>3); const int r19 = reg<8 ? 8*hf+reg : reg==10 ? 18 : (reg<10 && !hf) ? 8+reg : 19;
    const unsigned ap=ldsb+FR_KVB+r19*80+h*40;
    const u32x2 b0=*LDSP(u32x2,ap), b1=*LDSP(u32x2,ap+8), b2=*LDSP(u32x2,ap+16), b3=*LDSP(u32x2,ap+24); const u32x4 a0=cat(b0,b1), a1=cat(b2,b3); const unsigned a2=*LDSP(unsigned,ap+32);
    #pragma unroll
    for(int t=0;t<2;t++){ f32x16 z;
      asm volatile("s_waitcnt vmcnt(%3)" : "+v"(q0[t]), "+v"(q1[t]), "+v"(q2[t]) : "i"((t? 2:3)+SLA));
      mz32(z,a0,q0[t]); mv32<true>(z,a1,q1[t]); mv32<true>(z,u32x4{a2,0,0,0},u32x4{q2[t],0,0,0}); asm volatile("s_nop 7\n\ts_nop 3" : "+v"(z));
      const float rc=__builtin_amdgcn_rcpf(z[10]+1e-6f);
      const unsigned so=WS_OF+(tr*8+w*2+t)*36*1024;
      bstore128_sc1(u32x4{pk2(z[0]*rc,z[1]*rc),pk2(z[2]*rc,z[3]*rc),pk2(z[4]*rc,z[5]*rc),pk2(z[6]*rc,z[7]*rc)}, vl, srdws, so+head*1024);
      bstore32_sc1(pk2(z[8]*rc,z[9]*rc), h? 0x80000000u : vl, srdws, so+(32+(head>>3))*1024+((head>>2)&1)*512+(head&3)*4); }
    STAMPIT(17);
    SLAB(SLA,SLB)
    WAITV(SLB-SLA);
    flag_store(flags+(tr*4+w)*128+64+head, vtag);
    STAMPIT(2);
    SLAB(SLB,24)
    #undef SLAB
  }
  WST(0,2)
  poll_all(flags+tb*128+64, (lane&31)*4, w, tag);
  if(w==3) WAITV(0); else WAITV(24);
  BAR();
  if(blockIdx.x==0 && w==3) flag_store(flags+64*128+64, vtag);
  STAMPIT(3);
  {
    u32x4 o[18];
    { const unsigned vo=(g>>1)*1024+((g&1)*32+(w&1)*16+l15)*16, so=WS_OF+(tb*2+(w>>1))*36*1024;
      #define LD_(k) bload128<((k)&1)*2048,true>(o[k], vo, srdws, so+((k)>>1)*4096);
      LD_(0) LD_(1) LD_(2) LD_(3) LD_(4) LD_(5) LD_(6) LD_(7) LD_(8) LD_(9) LD_(10) LD_(11) LD_(12) LD_(13) LD_(14) LD_(15) LD_(16) LD_(17)
      #undef LD_
    }
    f32x4 acc[5];
    const unsigned wb=ldsb+FR_SLAB+(4*g+q)*160+p*8;
    u32x4 a[3][5];
    #define K2_RD(S) _Pragma("unroll") for(int j=0;j<5;j++) a[(S)%3][j]=trfrag(wb+(S)*5120+j*32, wb+(S)*5120+2560+j*32);
    K2_RD(0) K2_RD(1)
    #pragma unroll
    for(int S=0;S<18;S++){
      if(S+2<18) { K2_RD(S+2) }
      SB();
      asm volatile("s_waitcnt vmcnt(%1)" : "+v"(o[S]) : "i"(17-S));
      #pragma unroll
      for(int j=0;j<5;j++){ if(S==0) asm volatile("v_mfma_f32_16x16x32_bf16 %0, %1, %2, 0" : "=&v"(acc[j]) : "v"(a[0][j]), "v"(o[0])); else mv16(acc[j],a[S%3][j],o[S]); }
      SB(); }
    #undef K2_RD
    asm volatile("s_nop 7" : "+v"(acc[0]), "+v"(acc[1]), "+v"(acc[2]), "+v"(acc[3]), "+v"(acc[4]));
    BAR();
    STAMPIT(4);
    const float m=mx+row_bcast2(acc[4][0]+acc[4][1])*(1.f/D_);
    float s2=0.f;
    #pragma unroll
    for(int j=0;j<5;j++){ const float v0=acc[j][0]+(lo2f(xr[j][0])-m), v1=acc[j][1]+(hi2f(xr[j][0])-m), v2=acc[j][2]+(lo2f(xr[j][1])-m), v3=acc[j][3]+(hi2f(xr[j][1])-m);
      const int col=cg*72+16*j+4*g; const bool ok= j<4 || g<2;
      bstore64_sc1(u32x2{pk2(v0,v1),pk2(v2,v3)}, ok? (unsigned)((col>>5)*1024+(l15+16*((col>>2)&3))*16+((col>>4)&1)*8) : 0x80000000u, srdws, WS_HF+(tb*4+w)*18*1024);
      s2+= ok? v0*v0+v1*v1+v2*v2+v3*v3 : 0.f; }
    s2=rows_sum(s2);
    STAMPIT(19);
    bstore32_sc1(__float_as_uint(g? m:s2), g==0? (unsigned)(WS_SQ+((tb*8+cg)*64+l15*4+w)*4) : (g==1 && cg==0)? (unsigned)(WS_MB+(tb*64+w*16+l15)*4) : 0x80000000u, srdws, 0);
    WAITV(0);
    flag_store(flags+tb*128+96+cg*4+w, vtag);
    STAMPIT(5);
  }
  WST(2,6)
  #undef WST
  c.srdact=make_srd(ws+WS_ACT, S_*F_*2);
  const unsigned f2=2*(g&1)+(q>>1);
  const unsigned bfull=ldsb+(4*g+q)*128+(((2*fh)^f2)*32)+p*8, bhalf=ldsb+(4*g+q)*128+(((2*isB+fh)^f2)*32)+p*8;
  #pragma unroll
  for(int k=0;k<3;k++){ c.bA[k]=bfull+k*2*P1_SLOT; c.bB[k]=(bfull^32)+k*2*P1_SLOT; }
  c.stoff=lane*16+th*4096+fh*8192;
  const int fc=w&1, ch=isB;
  d.srdw=make_srd((const char*)wd+(size_t)xcd*576*1152-4096, 576*1152+4096);
  d.srda=make_srd(ws+WS_ACT+(size_t)(T*144+xcd*18)*8192-4096, 18*8192+4096);
  d.m0w=ldsb+w*P2_WREG; d.sowb=(unsigned)(8*w*1152); d.soab=(unsigned)(w*2048);
  auto kst=[&](int s){ if(s==12) return 8+isB; if(s==17) return 9-isB; const int z= s>12? s-1:s, pr=z>>1; const int who = pr<4 ? (pr&1) : (pr>=6); const int j = pr<4 ? (pr>>1) : 2+(pr&1);
    return 2*(((who^isB)?5:0)+((j+T)&3))+(z&1); };
  u64 tabA=0, tabB=0;
  #pragma unroll
  for(int s=1;s<6;s++) tabA|=(u64)kst(s)<<(5*(s-1));
  #pragma unroll
  for(int s=6;s<18;s++) tabB|=(u64)kst(s)<<(5*(s-6));
  asm volatile("" : "+s"(tabA), "+s"(tabB));
  d.sow=d.sowb+kst(0)*36864u; d.soa=d.soab+kst(0)*8192u; d.tab=tabA;
  d.myflag=flags+me*128; d.pflag=flags+(me^8)*128; d.tag=tag; d.vtag=vtag;
  poll_all(flags+T*256+96, (lane&31)*4+(lane>>5)*512, w, tag);
  BAR();
  STAMPIT(6);
  { u32x4 sv[2]; c.vl=lane*16; c.srdx=make_srd(ws+WS_HF+(size_t)(T*8+th*4)*18*1024, 4*18*1024);
    c.vwarm=(4096+((T*2+isB)*18+w*6)*1152+lane*128)|((lane<54 && w<3)? 0:0x80000000u);
    bload128<0,true>(sv[0], l15*16+g*512, srdws, WS_SQ+(T*2+th)*2048); bload128<256,true>(sv[1], l15*16+g*512, srdws, WS_SQ+(T*2+th)*2048);
    #define XL(k) p1_xload<k,0>(c); p1_xload<k,1>(c); p1_xload<k,2>(c); p1_xload<k,3>(c);
    XL(0) XL(1) XL(2) XL(3) XL(4) XL(5)
    #undef XL
    WAITV(12);
    asm volatile("" : "+v"(sv[0]), "+v"(sv[1]));
    #pragma unroll
    for(int t=0;t<4;t++){ const float v=rows_sum(__uint_as_float(sv[0][t])+__uint_as_float(sv[1][t]))*(1.f/D_)+1e-5f;
      c.ir2[t]=v; c.c1[t]=-1.4426950408889634f*__builtin_amdgcn_rsqf(v); asm volatile("" : "+v"(c.c1[t]), "+v"(c.ir2[t])); } }
  BAR();
  STAMPIT(7);
  #pragma unroll
  for(int k=0;k<2;k++){ trread<0>(c.flo[k][0],c.bA[0]+k*4096); trread<2048>(c.fhi[k][0],c.bA[0]+k*4096); trread<P1_REG>(c.flo[k][1],c.bA[0]+k*4096); trread<P1_REG+2048>(c.fhi[k][1],c.bA[0]+k*4096); }
  unsigned utab=unit_of(0)|unit_of(1)<<4|unit_of(2)<<8|unit_of(3)<<12|4<<16; const unsigned sob=xcd*9*128+rowoff, stb=(T*144+xcd*18)*8192;
  asm volatile("" : "+s"(utab));
  c.so_next=sob+((utab>>4)&15)*128;
  p1_unit<0>(c, d, 0);
  c.st_prev=stb+(utab&15)*16384; c.so_cur=c.so_next; utab>>=4;
  STAMPIT(15);
  #pragma unroll 1
  for(int it=1;it<4;it++){
    const unsigned un=utab&15; utab>>=4;
    c.so_next=sob+(utab&15)*128; d.f1=d.myflag+(utab>>4? 2:0);
    p1_unit<1>(c, d, bfull^(it==3? bfull^bhalf : 0));
    c.st_prev=stb+un*16384; c.so_cur=c.so_next;
    STAMPV(it==1? 18:30);
  }
  asm volatile("" :: "v"(c.vwarm));
  STAMPIT(8);
  int ln2=threadIdx.x&63; asm volatile("" : "+v"(ln2));
  { const int cc=ln2&7; const unsigned v0=(ln2>>3)*1152+(((cc>>1)^((ln2>>4)&3))*32)+(cc&1)*16+ch*640+4096;
    #pragma unroll
    for(int j=0;j<4;j++) d.vw[j]=v0-j*896;
    const int c4=ln2&3, r=(ln2>>2)&7; d.vw[4]= ln2<32? r*1152+512+ch*64+(((c4>>1)^((r>>2)&1))*32)+(c4&1)*16+4096 : 0x80000000u;
    d.va=ln2*16+4096-512; }
  p1_unit<2>(c, d, 0);
  c.st_prev=(unsigned)((T*144+xcd*18+8+isB)*8192);
  STAMPIT(9);
  #pragma unroll
  for(int j=0;j<7;j++) p2_issue<4>(d,j);
  p2_next(d); d.tab=tabB;
  { asm volatile("" : "+v"(ln2)); const int lane=ln2, q=(lane&15)>>2, p=lane&3; const int g=lane>>4; const int f=2*(g&1)+(q>>1);
    c.stoff=lane*16+th*4096+fh*8;
    #pragma unroll
    for(int sp=0;sp<3;sp++){
      #pragma unroll
      for(int jj=0;jj<4;jj++) d.ba[sp][jj]=ldsb+sp*2*P2_SLOT+(g>>1)*P2_WREG+fc*2048+(4*(g&1)+q)*128+8*p+((jj^f)*32);
      d.ba[sp][4]=ldsb+sp*2*P2_SLOT+(g>>1)*P2_WREG+4096+(4*(g&1)+q)*64+8*p+((fc^(g&1))*32);
      d.bb[sp]=ldsb+sp*2*P2_SLOT+2*th*P2_WREG+4608+lane*16; } }
  ldsread128a<0>(d.bf[0][0],d.bb[0]); ldsread128a<1024>(d.bf[0][1],d.bb[0]); ldsread128a<P2_WREG>(d.bf[0][2],d.bb[0]); ldsread128a<P2_WREG+1024>(d.bf[0][3],d.bb[0]);
  trread<0>(d.flo[0],d.ba[0][0]); trread<2*P2_WREG>(d.fhi[0],d.ba[0][0]); trread<0>(d.flo[1],d.ba[0][1]); trread<2*P2_WREG>(d.fhi[1],d.ba[0][1]); trread<0>(d.flo[2],d.ba[0][2]); trread<2*P2_WREG>(d.fhi[2],d.ba[0][2]);
  p2_stage<0,true>(d,c);
  STAMPIT(29);
  #pragma unroll 1
  for(int s=0;s<2;s++){ d.f2=d.pflag+s; p2_stage<1,false>(d,c); p2_stage<2,false>(d,c); p2_stage<3,false>(d,c); p2_stage<4,false>(d,c); p2_stage<5,false>(d,c); p2_stage<0,false>(d,c); STAMPV(s? 30:31); }
  STAMPIT(10);
  asm volatile("" : "+v"(ln2));
  { const int lane=ln2, l15=lane&15, g=lane>>4;
  d.srdp=make_srd(ws+WS_PART, 8*S_*D_*2); d.vl=lane*16; d.ownl=ldsb+lane*16;
  #pragma unroll
  for(int u=0;u<2;u++){     unsigned o; asm volatile("s_cmp_eq_u32 %2, %3\n\ts_cselect_b32 %0, 0x80000000, %4\n\ts_cselect_b32 %1, 0, 9216" : "=&s"(d.pb[u]), "=&s"(o) : "s"(2*w+u), "s"(xcd), "s"((unsigned)(((((T*2+ch)*8+2*w+u)*8+xcd)*9)*1024)) : "scc");
    d.ownw[u]=d.ownl+o; }
  p2_tgroup<0>(d); p2_tgroup<1>(d);
  const int ow=xcd>>1, ou=xcd&1; const int t16=T*8+(ow>>1)*4+2*ou;
  u32x4 hr[2]; unsigned h8, mA, m8;
  const int ks=ch*10+(ow&1)*4+w;
  bload128<0,true>(hr[0], d.vl, srdws, WS_HF+(t16*18+ks)*1024); bload128<0,true>(hr[1], d.vl, srdws, WS_HF+((t16+1)*18+ks)*1024);
  bload32<0,true>(h8, ((lane>>1)&1)*18*1024+(16*w+(lane>>2))*16+(lane&1)*4, srdws, WS_HF+(t16*18+8+ch)*1024+(ow&1)*8);
  bload32<0,true>(mA, l15*4+(lane>>5)*64, srdws, WS_MB+t16*64); bload32<0,true>(m8, (((lane>>1)&1)*16+(lane>>2))*4, srdws, WS_MB+t16*64);
  p2_tgroup<2>(d);
  STAMPIT(11);
  asm volatile("s_nop 7" ::: "memory");
  p2_dump_all<0>(d);
  asm volatile("s_waitcnt vmcnt(0) lgkmcnt(0)" ::: "memory");
  asm volatile("s_mov_b64 exec, 3\n\tglobal_store_dword %0, %1, %2 sc1\n\ts_mov_b64 exec, -1" :: "v"(lane*512), "v"(d.vtag), "s"(flags+(((me&~7)+2*w)*128+32+xcd)) : "memory");
  STAMPIT(12);
  BAR();
  asm volatile("" : "+v"(hr[0]), "+v"(hr[1]), "+v"(h8), "+v"(mA), "+v"(m8));
  f32x16 z[2]; float s8[2]; u32x4 idn;
  { const int r=lane&31; const unsigned one= (r>>3)==(lane>>5)? ((r&1)? 0x3f800000u:0x3f80u) : 0u; const int e=(r&7)>>1; idn=u32x4{e==0?one:0u,e==1?one:0u,e==2?one:0u,e==3?one:0u};
    z[0]=(f32x16)__uint_as_float(mA); z[1]=z[0];
    mv32<true>(z[0],idn,u32x4{hr[0][0],hr[0][1],hr[1][0],hr[1][1]}); mv32<true>(z[1],idn,u32x4{hr[0][2],hr[0][3],hr[1][2],hr[1][3]});
    s8[0]=lo2f(h8)+__uint_as_float(m8); s8[1]=hi2f(h8)+__uint_as_float(m8); }
  u32x4 v[8][2]; unsigned v8[8];
  #pragma unroll
  for(int s=1;s<8;s++) asm volatile("" : "=v"(v[s][0]), "=v"(v[s][1]), "=v"(v8[s]));
  { const unsigned sb=(unsigned)(((T*2+ch)*8+xcd)*8*9*1024);
    unsigned todo=0xfe;
    for(int n=0; n<0x100000 && todo; n++){
      unsigned f; asm volatile("global_load_dword %0, %1, %2 sc1\n\ts_waitcnt vmcnt(0)" : "=v"(f) : "v"(((lane+xcd)&7)*4), "s"(flags+me*128+32) : "memory");
      const unsigned go= n<0xfffff? (unsigned)__builtin_amdgcn_ballot_w64(f==tag)&todo : todo;
      #pragma unroll
      for(int s=1;s<8;s++) if(go>>s&1){ const unsigned so=sb+((xcd+s)&7)*9*1024;
        asm volatile("buffer_load_dwordx4 %0, %3, %5, %6 offen sc1\n\tbuffer_load_dwordx4 %1, %3, %5, %6 offen offset:1024 sc1\n\tbuffer_load_dword %2, %4, %5, %7 offen sc1"
                     : "+v"(v[s][0]), "+v"(v[s][1]), "+v"(v8[s]) : "v"(d.vl), "v"(lane*4), "s"(d.srdp), "s"(so+w*2048), "s"(so+8192+w*256) : "memory"); }
      todo&=~go; }
    STAMPIT(13);
    v[0][0]=*LDSP(u32x4,d.ownl+w*2048); v[0][1]=*LDSP(u32x4,d.ownl+w*2048+1024); v8[0]=*LDSP(unsigned,ldsb+8192+w*256+lane*4); }
  STAMPIT(20);
  #pragma unroll
  for(int x_=0;x_<8;x_++){
    if(x_) asm volatile("s_waitcnt vmcnt(0)" : "+v"(v[x_][0]), "+v"(v[x_][1]), "+v"(v8[x_]));
    mv32<false>(z[0],idn,v[x_][0]); mv32<false>(z[1],idn,v[x_][1]);
    s8[0]=dot2(v8[x_],selL,s8[0]); s8[1]=dot2(v8[x_],selH,s8[1]); }
  MPAD(z[0],z[1]);
  STAMPIT(21);
  { u16* o=out+(size_t)(t16*16+l15+16*(lane>>5))*D_+ks*32+4*(g&1);
    #pragma unroll
    for(int k=0;k<2;k++){ OUTST(u32x2,o+16*k,(u32x2{pk2(z[k][0],z[k][1]),pk2(z[k][2],z[k][3])})); OUTST(u32x2,o+16*k+8,(u32x2{pk2(z[k][4],z[k][5]),pk2(z[k][6],z[k][7])})); }
    OUTST(unsigned,out+(size_t)(t16*16+((lane>>1)&1)*16+(lane>>2))*D_+256+32*ch+16*(ow&1)+4*w+2*(lane&1),pk2(s8[0],s8[1])); }
  }
  STAMPIT(14); STAMP_END(15);
}
extern "C" void kernel_launch(void* const* d_in, const int* in_sizes, int n_in, void* d_out, int out_size, void* d_ws, size_t ws_size, hipStream_t stream){
  const u16* x=(const u16*)d_in[0]; const u16* wqkv=(const u16*)d_in[1]; const u16* wphi=(const u16*)d_in[2]; const u16* wo=(const u16*)d_in[3];
  const u16* wg=(const u16*)d_in[5]; const u16* wu=(const u16*)d_in[6]; const u16* wd=(const u16*)d_in[7];
  char* ws=(char*)d_ws+((ws_size-WS_END)&~(size_t)0xfffff);
  (void)hipFuncSetAttribute((const void*)k3, hipFuncAttributeMaxDynamicSharedMemorySize, K_LDS);
  k3<<<256,256,K_LDS,stream>>>(x,wqkv,wphi,wo,wg,wu,wd,ws,(u16*)d_out STAMP_PASS(0));
}
```

```cpp
#include <hip/hip_runtime.h>
#include <stdint.h>
#include <utility>
typedef unsigned short u16;
typedef unsigned long long u64;
using u32x2=__attribute__((ext_vector_type(2)))unsigned;
using u32x4=__attribute__((ext_vector_type(4)))unsigned;
using f32x4=__attribute__((ext_vector_type(4)))float;
using f32x16=__attribute__((ext_vector_type(16)))float;
#define DI __device__ __forceinline__
constexpr int S_=2048, D_=576, H_=32, DH_=18, F_=4608;
DI float bf2f(u16 v){ return __uint_as_float(((unsigned)v)<<16); }
DI u16 f2bf(float f){ unsigned u=__float_as_uint(f); u += 0x7fff + ((u>>16)&1); return (u16)(u>>16); }
template<unsigned M,int SH> DI unsigned divm(unsigned x){ return __umul24(x,M)>>SH; }
DI unsigned mad24(unsigned a,unsigned b,unsigned c){ return __umul24(a,b)+c; }
DI unsigned rfl(unsigned x){ return __builtin_amdgcn_readfirstlane(x); }
DI u32x4 make_srd(const void* p, unsigned bytes){ u64 a=(u64)p; u32x4 s; s[0]=rfl((unsigned)a); s[1]=rfl((unsigned)(a>>32)&0xffffu); s[2]=bytes; s[3]=0x20000u; return s; }
#define WAITL(n) __builtin_amdgcn_s_waitcnt(0xC07F|((n)<<8))
#define WAITV(n) __builtin_amdgcn_s_waitcnt(0x0F70|((n)&15)|(((n)>>4)<<14))
#define BAR() asm volatile("s_barrier" ::: "memory")
template<int OFF> DI void trread(u32x2& d, unsigned addr){ asm volatile("ds_read_b64_tr_b16 %0, %1 offset:%2" : "=v"(d) : "v"(addr), "i"(OFF)); }
template<int OFF> DI void ldsread128(u32x4& d, unsigned addr){ asm volatile("ds_read_b128 %0, %1 offset:%2" : "=v"(d) : "v"(addr), "i"(OFF)); }
template<int OFF> DI void ldsread128f(f32x4& d, unsigned addr){ asm volatile("ds_read_b128 %0, %1 offset:%2" : "=v"(d) : "v"(addr), "i"(OFF)); }
DI u32x4 cat(const u32x2& a,const u32x2& b){ return __builtin_shufflevector(a,b,0,1,2,3); }
DI void glds_m0(unsigned m0, unsigned voff, u32x4 srd, unsigned soff){
  asm volatile("s_mov_b32 m0, %0\n\ts_nop 0\n\tbuffer_load_dwordx4 %1, %2, %3 offen lds" :: "s"(m0), "v"(voff), "s"(srd), "s"(soff) : "memory");
}
template<int OFF> DI void glds_off(unsigned voff, u32x4 srd, unsigned soff){
  asm volatile("buffer_load_dwordx4 %0, %1, %2 offen offset:%3 lds" :: "v"(voff), "s"(srd), "s"(soff), "i"(OFF) : "memory");
}
DI void bstore128(u32x4 v, unsigned voff, u32x4 srd, unsigned soff){
  asm volatile("buffer_store_dwordx4 %0, %1, %2, %3 offen\n\ts_nop 1" :: "v"(v), "v"(voff), "s"(srd), "s"(soff) : "memory");
}
DI void vfma(float& d, float a, float b, float c){ asm volatile("v_fma_f32 %0, %1, %2, %3" : "=v"(d) : "v"(a), "v"(b), "v"(c)); }
DI void vmul(float& d, float a, float b){ asm volatile("v_mul_f32 %0, %1, %2" : "=v"(d) : "v"(a), "v"(b)); }
DI void vexp(float& d){ asm volatile("v_exp_f32 %0, %0" : "+v"(d)); }
DI void vrcp(float& d){ asm volatile("v_rcp_f32 %0, %0" : "+v"(d)); }
DI void vcvtpk(unsigned& d, float a, float b){ asm volatile("v_cvt_pk_bf16_f32 %0, %1, %2" : "=v"(d) : "v"(a), "v"(b)); }

constexpr unsigned WS_FQ=0, WS_KVP=WS_FQ+64*32*2304, WS_OF=WS_KVP+32*32*1536, WS_HF=WS_OF+S_*D_*2, WS_SQ=WS_HF+S_*D_*2, WS_OL=WS_SQ+32*8*4*128,
  WS_WOROW=WS_OL+S_*32*4, WS_SMALL=WS_WOROW+8192, WS_FLAGS=WS_SMALL, WS_ACT=WS_FLAGS+256*512, WS_PART=WS_ACT+S_*F_*2, WS_END=WS_PART+8*S_*D_*2;
using s16x4=__attribute__((ext_vector_type(4)))short;
using bf16x8=__attribute__((ext_vector_type(8)))__bf16;
using f32x2=__attribute__((ext_vector_type(2)))float;
using bf16x2=__attribute__((ext_vector_type(2)))__bf16;
#define LDSP(T,a) ((__attribute__((address_space(3))) T*)(a))
DI u32x2 trb(unsigned a){ return __builtin_bit_cast(u32x2, __builtin_amdgcn_ds_read_tr16_b64_v4i16(LDSP(s16x4,a))); }
DI u32x4 trfrag(unsigned a0, unsigned a1){ return cat(trb(a0),trb(a1)); }
DI f32x16 mfma32(u32x4 a,u32x4 b,f32x16 c){ return __builtin_amdgcn_mfma_f32_32x32x16_bf16(__builtin_bit_cast(bf16x8,a),__builtin_bit_cast(bf16x8,b),c,0,0,0); }
DI f32x4 mfma16(u32x4 a,u32x4 b,f32x4 c){ return __builtin_amdgcn_mfma_f32_16x16x32_bf16(__builtin_bit_cast(bf16x8,a),__builtin_bit_cast(bf16x8,b),c,0,0,0); }
DI unsigned pk2(float lo,float hi){ f32x2 v={lo,hi}; return __builtin_bit_cast(unsigned,__builtin_convertvector(v,bf16x2)); }
DI float lo2f(unsigned v){ return __uint_as_float(v<<16); }
DI float hi2f(unsigned v){ return __uint_as_float(v&0xffff0000u); }
DI float xhalf_max(float v){ auto r=__builtin_amdgcn_permlane32_swap(__float_as_uint(v),__float_as_uint(v),false,false); return fmaxf(__uint_as_float(r[0]),__uint_as_float(r[1])); }
DI float xhalf_min(float v){ auto r=__builtin_amdgcn_permlane32_swap(__float_as_uint(v),__float_as_uint(v),false,false); return fminf(__uint_as_float(r[0]),__uint_as_float(r[1])); }
DI float xhalf_sum(float v){ auto r=__builtin_amdgcn_permlane32_swap(__float_as_uint(v),__float_as_uint(v),false,false); return __uint_as_float(r[0])+__uint_as_float(r[1]); }
DI u32x4 packfrag(const f32x16& a,int s){ return u32x4{pk2(a[8*s],a[8*s+1]),pk2(a[8*s+2],a[8*s+3]),pk2(a[8*s+4],a[8*s+5]),pk2(a[8*s+6],a[8*s+7])}; }
DI void hedgehog(const f32x16& p, float r, unsigned (&o)[9]){
  float mx=fmaxf(fmaxf(p[0],p[1]),p[2]), mn=fminf(fminf(p[0],p[1]),p[2]);
  #pragma unroll
  for(int i=3;i<9;i+=2){ mx=fmaxf(fmaxf(mx,p[i]),p[i+1]); mn=fminf(fminf(mn,p[i]),p[i+1]); }
  mx=xhalf_max(mx); mn=xhalf_min(mn);
  const float L=1.4426950408889634f*r; const f32x2 LL={L,-L}, ab={-mx*L,mn*L};
  f32x2 e[9], s={0.f,0.f};
  #pragma unroll
  for(int i=0;i<9;i++){ const f32x2 t=__builtin_elementwise_fma(f32x2{p[i],p[i]},LL,ab); e[i]=f32x2{__builtin_amdgcn_exp2f(t[0]),__builtin_amdgcn_exp2f(t[1])}; s= i? s+e[i] : e[i]; }
  s=f32x2{__builtin_amdgcn_rcpf(xhalf_sum(s[0])),__builtin_amdgcn_rcpf(xhalf_sum(s[1]))};
  #pragma unroll
  for(int i=0;i<9;i++){ const f32x2 t=e[i]*s; o[i]=pk2(t[0],t[1]); }
}
template<bool P> DI void mv32(f32x16& d, const u32x4& a, const u32x4& b){
  if constexpr (P) asm volatile("s_nop 1\n\tv_mfma_f32_32x32x16_bf16 %0, %1, %2, %0" : "+v"(d) : "v"(a), "v"(b)); else asm volatile("v_mfma_f32_32x32x16_bf16 %0, %1, %2, %0" : "+v"(d) : "v"(a), "v"(b)); }
DI void mz32(f32x16& d, const u32x4& a, const u32x4& b){ asm volatile("s_nop 1\n\tv_mfma_f32_32x32x16_bf16 %0, %1, %2, 0" : "=&v"(d) : "v"(a), "v"(b)); }
#define MPAD(a,b) asm volatile("s_nop 7\n\ts_nop 3" : "+v"(a), "+v"(b))
template<int CTRL> DI float dpp_add(float v){ return v+__builtin_bit_cast(float,__builtin_amdgcn_update_dpp(0,__builtin_bit_cast(int,v),CTRL,0xf,0xf,true)); }
DI float dot2(unsigned a,unsigned b,float c){ return __builtin_amdgcn_fdot2_f32_bf16(__builtin_bit_cast(bf16x2,a),__builtin_bit_cast(bf16x2,b),c,false); }
constexpr int K1_RING=73728, K1_SLOT=13824, K1_PAIR=28672, K1_WPHI=K1_RING+2*K1_PAIR+K1_SLOT+2048, K1_STAT=K1_RING+3*K1_PAIR, K_LDS=K1_STAT+4096;
constexpr int k1_slot(int s){ return (s>>1)*K1_PAIR+(s&1)*K1_SLOT; }
DI void mv16(f32x4& d, const u32x4& a, const u32x4& b){ asm volatile("v_mfma_f32_16x16x32_bf16 %0, %1, %2, %0" : "+v"(d) : "v"(a), "v"(b)); }
#define SB() __builtin_amdgcn_sched_barrier(0)
constexpr int P1_SLOT=24576, P1_REG=12288;
constexpr int P2_WREG=6656, P2_SLOT=4*P2_WREG;
constexpr int FR_SLAB=53248, FR_KVB=FR_SLAB+576*160;
#define MAP 1
#define CODE_BYTES 46000
#define WQW 60
#define NOFW 0
#define SLA 4
#define SLB 12
#define OUTST(T,p,v) *(T*)(p)=v
#define W1R 12
#define W2S 0
#define NOPT 0
#define GAS __attribute__((address_space(1)))
struct P1 {
  u32x4 x[18][4];
  f32x4 acc[2][2][4];
  float tz[2][4][4], tp[2][4][4];
  u32x2 flo[3][2], fhi[3][2];
  unsigned pk[2][4];
  float c1[4], ir2[4];
  unsigned bA[3], bB[3];
  unsigned voff[2], stoff, vl, vwarm;
  u32x4 srdw, srdact, srdx;
  unsigned m0base, so_cur, so_next, st_prev;
};
struct P2 {
  f32x4 acc[9][4];
  u32x2 flo[6], fhi[6];
  u32x4 bf[2][4], bt[4][4];
  unsigned ba[3][5], bb[3];
  unsigned vw[5], va;
  u32x4 srdw, srda, srdp;
  unsigned m0w, sowb, soab, sow, soa, kk;
  u64 tab;
  GAS unsigned* myflag; GAS unsigned* pflag; GAS unsigned* f1; GAS unsigned* f3; unsigned pv, pv3, tag, vtag;
  unsigned r[4], vl, ownl, pb[2], ownw[2], vl8, pb8[2][2], ow8[2][2];
};
DI void bstore128_sc1(u32x4 v, unsigned voff, u32x4 srd, unsigned soff){
  asm volatile("buffer_store_dwordx4 %0, %1, %2, %3 offen sc1\n\ts_nop 1" :: "v"(v), "v"(voff), "s"(srd), "s"(soff) : "memory");
}
DI void bstore64_sc1(u32x2 v, unsigned voff, u32x4 srd, unsigned soff){
  asm volatile("buffer_store_dwordx2 %0, %1, %2, %3 offen sc1" :: "v"(v), "v"(voff), "s"(srd), "s"(soff) : "memory");
}
DI void bstore32_sc1(unsigned v, unsigned voff, u32x4 srd, unsigned soff){
  asm volatile("buffer_store_dword %0, %1, %2, %3 offen sc1" :: "v"(v), "v"(voff), "s"(srd), "s"(soff) : "memory");
}
template<int OFF> DI void glds_off_sc1(unsigned voff, u32x4 srd, unsigned soff){
  asm volatile("buffer_load_dwordx4 %0, %1, %2 offen offset:%3 sc1 lds" :: "v"(voff), "s"(srd), "s"(soff), "i"(OFF) : "memory");
}
template<int OFF> DI void set_m0(unsigned base){ asm volatile("s_add_u32 m0, %0, %1" :: "s"(base), "i"(OFF) : "scc", "memory"); }
template<int OFF> DI void glds_half(unsigned voff, u32x4 srd, unsigned soff){
  asm volatile("buffer_load_dwordx4 %0, %1, %2 offen offset:%3 lds" :: "v"(voff), "s"(srd), "s"(soff), "i"(OFF) : "memory");
}
template<int OFF> DI void ldsread128a(u32x4& d, unsigned addr){ asm volatile("ds_read_b128 %0, %1 offset:%2" : "=a"(d) : "v"(addr), "i"(OFF)); }
template<int OFF,bool SC1> DI void bload128(u32x4& d, unsigned voff, u32x4 srd, unsigned soff){
  if constexpr (SC1) asm volatile("buffer_load_dwordx4 %0, %1, %2, %3 offen offset:%4 sc1" : "=v"(d) : "v"(voff), "s"(srd), "s"(soff), "i"(OFF) : "memory");
  else asm volatile("buffer_load_dwordx4 %0, %1, %2, %3 offen offset:%4" : "=v"(d) : "v"(voff), "s"(srd), "s"(soff), "i"(OFF) : "memory");
}
template<int OFF,bool SC1> DI void bload64(u32x2& d, unsigned voff, u32x4 srd, unsigned soff){
  if constexpr (SC1) asm volatile("buffer_load_dwordx2 %0, %1, %2, %3 offen offset:%4 sc1" : "=v"(d) : "v"(voff), "s"(srd), "s"(soff), "i"(OFF) : "memory");
  else asm volatile("buffer_load_dwordx2 %0, %1, %2, %3 offen offset:%4" : "=v"(d) : "v"(voff), "s"(srd), "s"(soff), "i"(OFF) : "memory");
}
template<int OFF,bool SC1> DI void bload32(unsigned& d, unsigned voff, u32x4 srd, unsigned soff){
  if constexpr (SC1) asm volatile("buffer_load_dword %0, %1, %2, %3 offen offset:%4 sc1" : "=v"(d) : "v"(voff), "s"(srd), "s"(soff), "i"(OFF) : "memory");
  else asm volatile("buffer_load_dword %0, %1, %2, %3 offen offset:%4" : "=v"(d) : "v"(voff), "s"(srd), "s"(soff), "i"(OFF) : "memory");
}
DI void flag_store(GAS unsigned* p, unsigned v){
  asm volatile("global_store_dword %0, %1, %2 sc1" :: "v"(0u), "v"(v), "s"(p) : "memory");
}
DI void flag_load(unsigned& d, GAS unsigned* p){ asm volatile("global_load_dword %0, %1, %2 sc1" : "=v"(d) : "v"(0u), "s"(p) : "memory"); }
DI void flag_check(unsigned& v, GAS unsigned* p, unsigned need){
  unsigned s_, n_;
  asm volatile("s_nop 0\n\tv_readfirstlane_b32 %0, %2\n\ts_cmp_eq_u32 %0, %5\n\ts_cbranch_scc1 .Ldone%=\n\ts_mov_b32 %1, 0x100000\n"
               ".Lspin%=:\n\ts_sleep 2\n\tglobal_load_dword %2, %3, %4 sc1\n\ts_waitcnt vmcnt(0)\n\tv_readfirstlane_b32 %0, %2\n\ts_cmp_eq_u32 %0, %5\n\ts_cbranch_scc1 .Ldone%=\n\t"
               "s_sub_u32 %1, %1, 1\n\ts_cbranch_scc0 .Lspin%=\n.Ldone%=:"
               : "=&s"(s_), "=&s"(n_), "+&v"(v) : "v"(0u), "s"(p), "s"(need) : "scc", "memory");
}
DI void poll_all(GAS unsigned* p, unsigned voff, unsigned skip, unsigned tag){
  unsigned v_, n_;
  asm volatile("s_cmp_lg_u32 %4, 0\n\ts_cbranch_scc1 .Ldone%=\n\ts_mov_b32 %1, 0x100000\n"
               ".Lspin%=:\n\tglobal_load_dword %0, %2, %3 sc1\n\ts_waitcnt vmcnt(0)\n\tv_cmp_ne_u32 vcc, %5, %0\n\ts_cbranch_vccz .Ldone%=\n\t"
               "s_sleep 1\n\ts_sub_u32 %1, %1, 1\n\ts_cbranch_scc0 .Lspin%=\n.Ldone%=:"
               : "=&v"(v_), "=&s"(n_) : "v"(voff), "s"(p), "s"(skip), "s"(tag) : "scc", "vcc", "memory");
}
template<int Z,bool AG> DI void p1_mfma(f32x4& acc, const u32x4& a, const u32x4& x){
  if constexpr (Z==2)  asm volatile("v_mfma_f32_16x16x32_bf16 %0, %1, %2, 0" : "=&v"(acc) : "v"(a), "a"(x));
  if constexpr (Z==1 && AG)  asm volatile("v_mfma_f32_16x16x32_bf16 %0, %1, %2, 0" : "+v"(acc) : "v"(a), "a"(x));
  if constexpr (!Z && AG) asm volatile("v_mfma_f32_16x16x32_bf16 %0, %1, %2, %0" : "+v"(acc) : "v"(a), "a"(x));
  if constexpr (!AG)      asm volatile("v_mfma_f32_16x16x32_bf16 %0, %1, %2, %0" : "+v"(acc) : "v"(a), "v"(x));
}
template<int TL,int I> DI void p1_dr(P1& c){
  constexpr int tt=(I>>2)&3, r=I&3;
  if constexpr (I<16) vmul(c.tz[TL][tt][r], c.acc[TL][0][tt][r], c.c1[tt]); else vmul(c.tp[TL][tt][r], c.acc[TL][0][tt][r], c.acc[TL][1][tt][r]);
}
constexpr int dr_cum(int G){ int n=0; for(int g=0;g<G;g++){ const int m=g&7; n+= m==6?0 : (m&1)?1:2; if(g==1||g==9) n++; } return n; }
template<int TL,int LO,int HI> DI void p1_drs(P1& c){ if constexpr (LO<HI){ p1_dr<TL,LO>(c); p1_drs<TL,LO+1,HI>(c);} }
template<int TT,int HF,int G> DI void p1_grp(P1& c){
  float* z=c.tz[HF][TT]; float* p=c.tp[HF][TT];
  if constexpr (G==0) vexp(z[0]); if constexpr (G==2) vexp(z[1]); if constexpr (G==4) vexp(z[2]); if constexpr (G==8) vexp(z[3]);
  if constexpr (G==5) vfma(z[0],z[0],c.ir2[TT],c.ir2[TT]); if constexpr (G==7) vfma(z[1],z[1],c.ir2[TT],c.ir2[TT]);
  if constexpr (G==9) vfma(z[2],z[2],c.ir2[TT],c.ir2[TT]); if constexpr (G==13) vfma(z[3],z[3],c.ir2[TT],c.ir2[TT]);
  if constexpr (G==10) vrcp(z[0]); if constexpr (G==12) vrcp(z[1]); if constexpr (G==16) vrcp(z[2]); if constexpr (G==18) vrcp(z[3]);
  if constexpr (G==15) vmul(p[0],p[0],z[0]); if constexpr (G==17) vmul(p[1],p[1],z[1]); if constexpr (G==21) vmul(p[2],p[2],z[2]); if constexpr (G==23) vmul(p[3],p[3],z[3]);
}
template<int TT,int HF,int G> DI void p1_cvt(P1& c){ float* p=c.tp[HF][TT];
  if constexpr (G==1) vcvtpk(c.pk[TT&1][2*HF],p[0],p[1]); if constexpr (G==3) vcvtpk(c.pk[TT&1][2*HF+1],p[2],p[3]); }
template<int TT> DI void p1_store(P1& c){ const unsigned* k=c.pk[TT&1]; asm volatile("buffer_store_dwordx4 %0, %1, %2, %3 offen sc1" :: "v"(u32x4{k[0],k[1],k[2],k[3]}), "v"(c.stoff), "s"(c.srdact), "s"(c.st_prev+TT*1024) : "memory"); }
template<int SL> DI void p2_issue(P2& d, int j){
  if(j==0) { set_m0<SL*P2_SLOT>(d.m0w); asm volatile("s_nop 0"); glds_off<0>(d.vw[0],d.srdw,d.sow); }
  if(j==1) glds_off<1024>(d.vw[1],d.srdw,d.sow);
  if(j==2) glds_off<2048>(d.vw[2],d.srdw,d.sow);
  if(j==3) { glds_off<3072>(d.vw[3],d.srdw,d.sow); set_m0<SL*P2_SLOT+4096>(d.m0w); }
  if(j==4) glds_half<0>(d.vw[4],d.srdw,d.sow);
  if(j==5) glds_off_sc1<512>(d.va,d.srda,d.soa);
  if(j==6) glds_off_sc1<1536>(d.va,d.srda,d.soa);
}
DI void p2_next(P2& d){ asm volatile("s_and_b32 %0, %3, 31\n\ts_lshr_b64 %2, %2, 5\n\ts_mul_i32 %1, %0, 0x9000\n\ts_lshl_b32 %0, %0, 13\n\ts_add_u32 %1, %1, %4\n\ts_add_u32 %0, %0, %5"
   : "=&s"(d.soa), "=&s"(d.sow), "+s"(d.tab) : "s"((unsigned)d.tab), "s"(d.sowb), "s"(d.soab) : "scc"); }
template<int ST,int PC> DI void p1_glds(P1& c){ constexpr int SL=(ST+5)%6; const unsigned so=(ST==0? c.so_cur : c.so_next) + (unsigned)((96*SL+8*PC)*9216-(PC&3)*1024); glds_off<(PC&3)*1024>(c.voff[PC&1], c.srdw, so); }
template<int K,int TT> DI void p1_xload(P1& c){
  if constexpr (K<16) asm volatile("buffer_load_dwordx4 %0, %1, %2, %3 offen offset:%4 sc1" : "=a"(c.x[K][TT]) : "v"(c.vl), "s"(c.srdx), "s"((TT*18+(K&~3))*1024), "i"((K&3)*1024));
  else asm volatile("buffer_load_dwordx4 %0, %1, %2, %3 offen offset:%4 sc1" : "=v"(c.x[K][TT]) : "v"(c.vl), "s"(c.srdx), "s"((TT*18+(K&~3))*1024), "i"((K&3)*1024)); }
template<int ST,int TL,int KK,int M,int MODE> DI void p1_gap(P1& c, P2& d){
  constexpr bool FULL=MODE<2;
  constexpr int G=8*KK+M, SG=2*ST+TL, K=3*ST+KK;
  { const u32x4 a=cat(c.flo[KK][M>>2],c.fhi[KK][M>>2]); p1_mfma<(K==0? (MODE==0?2:1):0),(K<16)>(c.acc[TL][M>>2][M&3], a, c.x[K][M&3]); }
  if constexpr (M&1) {
    constexpr int N=(KK+2)%3; constexpr bool same=KK==0, toB= !same && TL==0 && FULL; constexpr int S2=(same||toB)? ST : (ST+1)%6;
    constexpr int IMM=(S2&1)*P1_SLOT+(M>>2)*P1_REG+N*4096+((M>>1)&1)*512;
    const unsigned b=((same&&TL)||toB)? c.bB[S2>>1] : c.bA[S2>>1];
    if constexpr (FULL || ST<5 || same) { if constexpr ((M>>1)&1) trread<IMM>(c.fhi[N][M>>2], b); else trread<IMM>(c.flo[N][M>>2], b); }
  }
  if constexpr (FULL) {
    constexpr int PC=3*TL+KK;
    if constexpr (MODE==1 || ST>0) {
      if constexpr (M==4 && (PC==0||PC==4)) set_m0<((ST+5)%6)*P1_SLOT+PC*1024>(c.m0base);
      if constexpr (M==6) p1_glds<ST,PC>(c); }
    if constexpr (SG==11||(SG==0&&MODE==1)) p1_drs<(SG==0),dr_cum(G),dr_cum(G+1)>(c);
    if constexpr (MODE==1) {
      if constexpr (SG>=1 && SG<=8) p1_grp<((SG-1)>>1),((SG-1)&1),G>(c);
      if constexpr (SG>=2 && SG<=9) p1_cvt<((SG-2)>>1),((SG-2)&1),G>(c);
      if constexpr (G==20 && (SG==3||SG==5||SG==7||SG==9)) p1_store<(SG-3)/2>(c);
    } else if constexpr (MODE==0) {
      if constexpr (ST<4 && (M==0||M==2)) p1_xload<3*(ST+2)+(PC>>1),2*(PC&1)+(M>>1)>(c);
      if constexpr (G==20 && SG==9) asm volatile("buffer_load_dword %0, %0, %1, 0 offen" : "+v"(c.vwarm) : "s"(d.srdw) : "memory");
    }
  } else {
    if constexpr (ST==0) { p1_drs<1,dr_cum(G),dr_cum(G+1)>(c);
      if constexpr (M==1 && KK!=1) set_m0<5*P1_SLOT+KK*2048>(c.m0base);
      if constexpr (M==2) p1_glds<0,2*KK>(c); if constexpr (M==6) p1_glds<0,2*KK+1>(c); }
    if constexpr (ST>=1 && ST<=4) { p1_grp<ST-1,0,G>(c); p1_grp<ST-1,1,(G^1)>(c); }
    if constexpr (ST>=2) { p1_cvt<ST-2,0,G>(c); p1_cvt<ST-2,1,(G^1)>(c); }
    if constexpr (ST>=2) { constexpr int j= G==6?0: G==7?1: G==14?2: G==15?3: G==20?4: G==22?5: G==23?6: -1; if constexpr (j>=0) p2_issue<ST-2>(d,j);
      if constexpr (G==21) p1_store<ST-2>(c); if constexpr (G==23) p2_next(d); }
  }
}
template<int ST,int TL,int KK,int FULL> DI void p1_step(P1& c, P2& d){
  constexpr bool LAST=FULL==2 && ST==5;
  if constexpr (FULL==0 && ST==0 && TL==0 && KK>0) WAITV(28-2*KK);
  WAITL(LAST&&KK==2? 2:6);
  p1_gap<ST,TL,KK,0,FULL>(c,d); p1_gap<ST,TL,KK,1,FULL>(c,d); p1_gap<ST,TL,KK,2,FULL>(c,d); p1_gap<ST,TL,KK,3,FULL>(c,d);
  WAITL(LAST&&KK==2? 0: LAST&&KK==1? 4:6);
  p1_gap<ST,TL,KK,4,FULL>(c,d); p1_gap<ST,TL,KK,5,FULL>(c,d); p1_gap<ST,TL,KK,6,FULL>(c,d); p1_gap<ST,TL,KK,7,FULL>(c,d);
}
template<int ST,int TL,int FULL> DI void p1_section(P1& c, P2& d){ p1_step<ST,TL,0,FULL>(c,d); p1_step<ST,TL,1,FULL>(c,d); p1_step<ST,TL,2,FULL>(c,d); }
template<int ST,int MODE> DI void p1_stage(P1& c, P2& d, unsigned bA_next){
  if constexpr (MODE==2 && ST==2) flag_load(d.pv,d.pflag);
  if constexpr (MODE==1 && ST==2) flag_store(d.f1,d.vtag);
  p1_section<ST,0,MODE>(c,d);
  if constexpr (ST==5 && MODE==1) {
    #pragma unroll
    for(int k=0;k<3;k++) c.bA[k]=bA_next+k*2*P1_SLOT; }
  if constexpr (MODE<2) p1_section<ST,1,MODE>(c,d);
  if constexpr (MODE==0) WAITV(ST==0?12: ST<4?18: ST==4?7:19);
  if constexpr (MODE==1) WAITV(ST<2?19: ST==2?20: ST<5?21:20);
  if constexpr (MODE==2) { if constexpr (ST==0) WAITV(19); if constexpr (ST==1) WAITV(12); if constexpr (ST==2) { WAITV(8); flag_check(d.pv,d.pflag,d.tag); } if constexpr (ST==5) WAITV(16); }
  BAR();
}
template<int MODE> DI void p1_unit(P1& c, P2& d, unsigned bn){ p1_stage<0,MODE>(c,d,bn); p1_stage<1,MODE>(c,d,bn); p1_stage<2,MODE>(c,d,bn); p1_stage<3,MODE>(c,d,bn); p1_stage<4,MODE>(c,d,bn); p1_stage<5,MODE>(c,d,bn); }
template<int I> DI void p1_fin(P1& c){
  if constexpr (I<104) { constexpr int tt=I/26, k=I%26, r=k&3; f32x4 &g=c.acc[0][0][tt], &u=c.acc[0][1][tt]; float t;
    if constexpr (k<4) { vmul(t,g[r],u[r]); u[r]=t; }
    else if constexpr (k<8) { vmul(t,g[r],c.c1[tt]); g[r]=t; }
    else if constexpr (k<12) { t=g[r]; vexp(t); g[r]=t; }
    else if constexpr (k<16) { vfma(t,g[r],c.ir2[tt],c.ir2[tt]); g[r]=t; }
    else if constexpr (k<20) { t=g[r]; vrcp(t); g[r]=t; }
    else if constexpr (k<24) { vmul(t,u[r],g[r]); u[r]=t; }
    else vcvtpk(c.pk[tt>>1][2*(tt&1)+k-24],u[2*(k-24)],u[2*(k-24)+1]); }
}
DI void p1_give(P1& c){ const bool f=c.st_prev&1; unsigned g[4];
  asm volatile("s_nop 0" : "+v"(c.pk[1][3]));
  #pragma unroll
  for(int k=0;k<4;k++){ g[k]= f? c.pk[0][k]:c.pk[1][k]; c.pk[0][k]= f? c.pk[1][k]:c.pk[0][k]; }
  *LDSP(u32x2,c.vl)=u32x2{g[0],g[1]}; *LDSP(u32x2,c.vl+512)=u32x2{g[2],g[3]}; WAITL(0); }
DI void p1_take(P1& c){ const bool f=c.st_prev&1; const u32x2 a=*LDSP(u32x2,c.vl^1024), b=*LDSP(u32x2,(c.vl^1024)+512); const unsigned* k=c.pk[0];
  bstore128_sc1(f? u32x4{a[0],a[1],k[0],k[1]} : u32x4{k[0],k[1],a[0],a[1]}, c.stoff, c.srdact, c.st_prev&~1u);
  bstore128_sc1(f? u32x4{b[0],b[1],k[2],k[3]} : u32x4{k[2],k[3],b[0],b[1]}, c.stoff, c.srdact, (c.st_prev&~1u)+1024); }
template<bool Z> DI void p2_mfma(f32x4& acc, const u32x4& a, const u32x4& b){
  if constexpr (Z) asm volatile("v_mfma_f32_16x16x32_bf16 %0, %1, %2, 0" : "=&v"(acc) : "v"(a), "a"(b));
  else asm volatile("v_mfma_f32_16x16x32_bf16 %0, %1, %2, %0" : "+v"(acc) : "v"(a), "a"(b)); }
template<bool E,int I> DI void p2_fin(P1& e){ if constexpr (E) { p1_fin<3*I>(e); p1_fin<3*I+1>(e); p1_fin<3*I+2>(e); } }
template<int P,int J,bool Z> DI void p2_step(P2& c, P1& e){
  constexpr int PAR=P&1, ST=PAR*9+J, CUR=ST%6, NX=(ST+3)%6; constexpr int JN=(J+3)%9; constexpr int PN=(J+3<9)?P:(P+1)%6;
  constexpr int N = J<3?4: J==3?5: J==4?6: J<7?7: J==7?6:5;
  constexpr int IMM=(PN&1)*P2_SLOT+(JN<8?(JN>>2)*1024:0); constexpr int BI=JN<8?(JN&3):4;
  constexpr int P1n=(P+1)%6, PF=(P+5)%6;
  WAITL(N);
  u32x4 a=cat(c.flo[CUR],c.fhi[CUR]);
  p2_mfma<Z>(c.acc[J][0],a,c.bf[PAR][0]);
  trread<IMM>(c.flo[NX], c.ba[PN>>1][BI]);
  p2_fin<Z,4*J>(e);
  p2_mfma<Z>(c.acc[J][1],a,c.bf[PAR][1]);
  trread<IMM+2*P2_WREG>(c.fhi[NX], c.ba[PN>>1][BI]);
  p2_fin<Z,4*J+1>(e);
  p2_mfma<Z>(c.acc[J][2],a,c.bf[PAR][2]);
  if constexpr (J>=2 && J<=5) ldsread128a<(P1n&1)*P2_SLOT+((J-2)>>1)*P2_WREG+((J-2)&1)*1024>(c.bf[PAR^1][J-2], c.bb[P1n>>1]);
  if constexpr (J==0) set_m0<PF*P2_SLOT>(c.m0w);
  if constexpr (J==7) asm volatile("s_bfe_u32 %0, %1, %2" : "=s"(c.kk) : "s"((unsigned)c.tab), "i"(5<<16) : "scc");
  if constexpr (J==8) asm volatile("s_mul_i32 %0, %1, 0x9000" : "=s"(c.sow) : "s"(c.kk));
  p2_fin<Z,4*J+2>(e);
  p2_mfma<Z>(c.acc[J][3],a,c.bf[PAR][3]);
  if constexpr (J<=3) glds_off<J*1024>(c.vw[J], c.srdw, c.sow);
  if constexpr (J==3) set_m0<PF*P2_SLOT+4096>(c.m0w);
  if constexpr (J==4) glds_half<0>(c.vw[4], c.srdw, c.sow);
  if constexpr (J==5) glds_off_sc1<512>(c.va, c.srda, c.soa);
  if constexpr (J==6) glds_off_sc1<1536>(c.va, c.srda, c.soa);
  if constexpr (J==7) { asm volatile("s_lshl_b32 %0, %1, 13\n\ts_lshr_b64 %2, %2, 5" : "=&s"(c.soa), "+s"(c.kk), "+s"(c.tab) :: "scc"); }
  if constexpr (J==8) { asm volatile("s_add_u32 %0, %0, %2\n\ts_add_u32 %1, %1, %3" : "+s"(c.sow), "+s"(c.soa) : "s"(c.sowb), "s"(c.soab) : "scc"); }
  p2_fin<Z,4*J+3>(e);
}
template<int P,bool Z> DI void p2_stage(P2& c, P1& e){
  if constexpr (P==3) flag_store(c.myflag+1,c.vtag);
  if constexpr (P==0 && !Z) flag_load(c.pv,c.pflag+1);
  if constexpr (P==4) flag_load(c.pv3,c.f3);
  if constexpr (P==5) flag_store(c.myflag+3,c.vtag);
  p2_step<P,0,Z>(c,e); p2_step<P,1,Z>(c,e); p2_step<P,2,Z>(c,e); p2_step<P,3,Z>(c,e); p2_step<P,4,Z>(c,e); p2_step<P,5,Z>(c,e); p2_step<P,6,Z>(c,e); p2_step<P,7,Z>(c,e); p2_step<P,8,Z>(c,e);
  if constexpr (Z) { p1_give(e); WAITV(22); } else if constexpr (P==2) WAITV(16); else if constexpr (P==5) { WAITV(15); flag_check(c.pv3,c.f3,c.tag); } else if constexpr (P==1) { WAITV(14); flag_check(c.pv,c.pflag+1,c.tag); } else WAITV(21);
  BAR();
}
template<int J,int U,int K> DI void p2_dump(P2& c){
  if constexpr (K<4) vcvtpk(c.r[K],c.acc[J][2*U+(K>>1)][2*(K&1)],c.acc[J][2*U+(K>>1)][2*(K&1)+1]);
  else if constexpr (J==8) {
    asm volatile("buffer_store_dwordx2 %0, %1, %2, %3 offen sc1\n\tds_write_b64 %4, %0" :: "v"(u32x2{c.r[0],c.r[1]}), "v"(c.vl8), "s"(c.srdp), "s"(c.pb8[U][0]), "v"(c.vl8+c.ow8[U][0]) : "memory");
    asm volatile("buffer_store_dwordx2 %0, %1, %2, %3 offen sc1\n\tds_write_b64 %4, %0" :: "v"(u32x2{c.r[2],c.r[3]}), "v"(c.vl8), "s"(c.srdp), "s"(c.pb8[U][1]), "v"(c.vl8+c.ow8[U][1]) : "memory"); }
  else asm volatile("buffer_store_dwordx4 %0, %1, %2, %3 offen sc1\n\tds_write_b128 %4, %0 offset:%5\n\ts_nop 0"
                    :: "v"(u32x4{c.r[0],c.r[1],c.r[2],c.r[3]}), "v"(c.vl), "s"(c.srdp), "s"(c.pb[U]+J*1024), "v"(c.ownw[U]), "i"(J*1024) : "memory");
}
template<int GI,int I> DI void p2_dumpq(P2& c){ if constexpr (GI>=0 && I>=0 && I<30) p2_dump<3*GI+I/10,(I/5)&1,I%5>(c); }
template<int GI,int S,int JJ> DI void p2_tstep(P2& c){
  constexpr int n=(GI*5+S)*3+JJ, CUR=(n+3)%6, NX=n%6, J=3*GI+JJ;
  constexpr int SN=S<4? S+1:0, JN=S<4? J : J+3, PN=SN+1;
  constexpr int IMM=(PN&1)*P2_SLOT+(JN<8?(JN>>2)*1024:0); constexpr int BI=JN<8?(JN&3):4;
  constexpr bool RD=n+3<45;
  if constexpr (GI==0) { if constexpr (JJ==0) WAITL(S==0?4:2); if constexpr (JJ==1) WAITL(S==0?6:S<4?8:4); if constexpr (JJ==2) WAITL(S<4?8:4); }
  else WAITL(n<43? 4 : n==43? 2:0);
  const u32x4 a=cat(c.flo[CUR],c.fhi[CUR]);
  const u32x4* b= S==0? c.bf[1] : c.bt[S-1<0?0:S-1];
  p2_mfma<false>(c.acc[J][0],a,b[0]);
  if constexpr (RD) trread<IMM>(c.flo[NX], c.ba[PN>>1][BI]);
  p2_mfma<false>(c.acc[J][1],a,b[1]);
  if constexpr (RD) trread<IMM+2*P2_WREG>(c.fhi[NX], c.ba[PN>>1][BI]);
  p2_mfma<false>(c.acc[J][2],a,b[2]);
  if constexpr (GI==0 && S<4 && JJ<2) ldsread128a<((S+2)&1)*P2_SLOT+JJ*P2_WREG>(c.bt[S][2*JJ], c.bb[(S+2)>>1]);
  p2_dumpq<GI-1,(S*3+JJ-1)*3>(c); p2_dumpq<GI-1,(S*3+JJ-1)*3+1>(c);
  p2_mfma<false>(c.acc[J][3],a,b[3]);
  if constexpr (GI==0 && S<4 && JJ<2) ldsread128a<((S+2)&1)*P2_SLOT+JJ*P2_WREG+1024>(c.bt[S][2*JJ+1], c.bb[(S+2)>>1]);
  p2_dumpq<GI-1,(S*3+JJ-1)*3+2>(c);
}
template<int GI,int S> DI void p2_tstage(P2& c){ p2_tstep<GI,S,0>(c); p2_tstep<GI,S,1>(c); p2_tstep<GI,S,2>(c);
  if constexpr (GI==0 && S<3) { WAITV(14-7*S); BAR(); } }
template<int GI> DI void p2_tgroup(P2& c){ p2_tstage<GI,0>(c); p2_tstage<GI,1>(c); p2_tstage<GI,2>(c); p2_tstage<GI,3>(c); p2_tstage<GI,4>(c); }
template<int I> DI void p2_dump_all(P2& c){ if constexpr (I<30){ p2_dumpq<2,I>(c); p2_dump_all<I+1>(c); } }
DI float row_bcast2(float v){
  auto a=__builtin_amdgcn_permlane32_swap(__float_as_uint(v),__float_as_uint(v),false,false);
  auto b=__builtin_amdgcn_permlane16_swap(a[1],a[1],false,false); return __uint_as_float(b[0]); }
DI float rows_sum(float v){ auto b=__builtin_amdgcn_permlane16_swap(__float_as_uint(v),__float_as_uint(v),false,false); return xhalf_sum(__uint_as_float(b[0])+__uint_as_float(b[1])); }
template<bool LO> DI unsigned slab_voff(int pc, int rr, int cc){
  const int R=6*pc+rr, S=R>>5, g=(R>>2)&3, j=4*((R>>4)&1)+(R&3), a=2*S+(g>>1), hp=g&1;
  const bool lo= LO || a<32; const unsigned row=mad24(18,lo? a : 8*(a-32)+4*hp+(j>>1),lo? 8*hp+j : 16+(j&1));
  return mad24(row,1152,cc*16); }
DI void slab_glds(unsigned m0, unsigned voff, u32x4 srd, unsigned soff){
  asm volatile("s_mov_b32 m0, %0\n\ts_mov_b64 exec, %4\n\tbuffer_load_dwordx4 %1, %2, %3 offen lds\n\ts_mov_b64 exec, -1" :: "s"(m0), "v"(voff), "s"(srd), "s"(soff), "s"(0x07FDFF7FDFF7FDFFull) : "memory"); }

__global__ __launch_bounds__(256,1) void k3(const u16* __restrict__ x, const u16* __restrict__ wqkv, const u16* __restrict__ wphi, const u16* __restrict__ wo, const u16* __restrict__ wg, const u16* __restrict__ wu, const u16* __restrict__ wd,
                                            char* __restrict__ ws, u16* __restrict__ out){
  extern __shared__ __attribute__((aligned(16))) char lds[];
  asm volatile("" :: "s"(x), "s"(wqkv), "s"(wphi), "s"(wo), "s"(wg), "s"(wu), "s"(wd), "s"(ws), "s"(out));
  const int tid=threadIdx.x, lane=tid&63; const int w=rfl(tid>>6);
  const int xq=blockIdx.x&7, i=blockIdx.x>>3, isB=i&1;
  const int xcd=(xq&(8-MAP))|((i>>1)&(MAP-1)), T=(xq&(MAP-1))*(16/MAP)+(i>>1)/MAP;
  const int tb=(xq>>1)*8+(i&7), cg=(xq&1)*4+(i>>3);
  const int me=(T*2+isB)*8+xcd;
  const unsigned ldsb=(unsigned)(size_t)lds;
  GAS unsigned* flags=(GAS unsigned*)(ws+WS_FLAGS);
  const u32x4 srdws=make_srd(ws, WS_ACT);
  const int g=lane>>4, l15=lane&15, q=l15>>2, p=lane&3;
  P1 c; P2 d;
  unsigned selL=0x3f80u, selH=0x3f800000u; asm volatile("" : "+s"(selL), "+s"(selH));
  const int th=w>>1, fh=w&1;
  c.srdw=make_srd(th? wu:wg, D_*F_*2);
  { const int cc=lane&7; c.voff[0]=(lane>>3)*9216+(((cc>>1)^((lane>>4)&1))*32)+(cc&1)*16; c.voff[1]=c.voff[0]^64; }
  c.m0base=ldsb+th*P1_REG+fh*6*1024;
  const unsigned rowoff=(unsigned)(fh*48)*9216u;
  auto unit_of=[&](int it){ return it<4 ? (isB?5:0)+((it+T)&3) : 4; };
  auto so_of=[&](int it){ return (unsigned)((xcd*9+unit_of(it))*128)+rowoff; };
  #define WST(lo,hi) if(w==1||w==2){ _Pragma("unroll 1") for(int s=lo;s<hi;s++) _Pragma("unroll") for(int j=0;j<12;j++) glds_m0(ldsb+th*P1_REG+s*P1_SLOT+j*1024, c.voff[j&1], c.srdw, so_of(0)-rowoff+(unsigned)(96*s+8*j)*9216u); }
  unsigned tag, vtag;
  const int h=lane>>5, blk=(lane>>4)&1; const unsigned vl=lane*16;
  const u32x4 srdo=make_srd(wo, D_*D_*2);
  unsigned sv[8], warm; u32x2 xr[5]; float mx;
  #define SLAB(lo,hi) _Pragma("unroll") for(int it=lo;it<hi;it++) slab_glds(ldsb+FR_SLAB+(w+4*it)*960, sv[it<20? it&3 : it-16], srdo, cg*144+(it<20?(it>>2)*108*1152:0));
  {
    const int tbA=tb, hg=cg, head=4*hg+w, off=2*(w&1);
    const u32x4 srdx=make_srd(x+(size_t)tbA*64*D_, 64*D_*2), srdw=make_srd(wqkv, D_*3*D_*2);
    const unsigned vx0=(lane>>3)*1152+(((lane&7)^(lane>>4))*16), vx1=(vx0^64)+8*1152;
    #define A_X(b) { glds_m0(ldsb+w*2048+(b)*8192, vx0, srdx, w*16*1152+(b)*128); glds_m0(ldsb+w*2048+(b)*8192+1024, vx1, srdx, w*16*1152+(b)*128); }
    A_X(0) A_X(1)
    flag_load(vtag, flags+64*128+64);
    u32x4 wr0, wr1; const unsigned worw = w*256+blockIdx.x;
    bload128<0,false>(wr0, vl, srdo, worw*1152); bload128<1024,false>(wr1, lane<8? vl:0x80000000u, srdo, worw*1152);
    asm volatile("s_mov_b32 m0, %0\n\ts_nop 0\n\tbuffer_load_dwordx4 %1, %3, 0 offen lds\n\tbuffer_load_dword %2, %3, 0 offen offset:640 lds"
                 :: "s"(ldsb+K1_WPHI+3712+w*1024), "v"(lane<40? vl:0x80000000u), "v"(lane*4), "s"(make_srd(wphi+head*324, 648)) : "memory");
    unsigned vw[7];
    #pragma unroll
    for(int k=0;k<7;k++){ const unsigned ck=(w+4*k)*64+lane, row=divm<2428,16>(ck), cc=ck-__umul24(row,27); vw[k]=mad24(row,3456,mad24(divm<57,9>(cc),1008,cc*16)); }
    vw[6]|= (unsigned)(w+1)<<29&0x80000000u;
    const unsigned sw0=hg*144, mw0=ldsb+K1_RING+w*1024;
    #define A_W(PR,LO,HI) _Pragma("unroll") for(int k=LO;k<HI;k++) glds_m0(mw0+(PR)*K1_PAIR+k*4096, vw[k], srdw, sw0+(PR)*64*3456);
    A_W(0,0,7)
    unsigned bx[4], bw[2];
    { const int row=lane&31;
      #pragma unroll
      for(int e=0;e<4;e++) bx[e]=ldsb+row*128+(((2*e+h)^((row>>1)&7))*16);
      const int hoff=36*w-2*off;
      #pragma unroll
      for(int t=0;t<2;t++){ unsigned gi=8*t+4*blk+p; gi=gi>14?14:gi; bw[t]=ldsb+K1_RING+mad24(8*h+q,432,mad24(divm<52,8>(gi),104,8*gi))+hoff; } }
    unsigned wdst=0;
    { const unsigned r9=divm<57,9>(lane); warm=mad24(r9,9216-1152,lane*128)|(lane<54? 0:0x80000000u); }
    f32x16 acc[2][2]={}; f32x4 cs4={}; float s1[2]={0,0}, s2[2]={0,0};
    u32x4 sel[2];
    #pragma unroll
    for(int t=0;t<2;t++){ const unsigned v= l15==2*t+(g&1)? 0x3f803f80u:0u; sel[t]=u32x4{v,v,v,v}; }
    unsigned wa[8];
    #pragma unroll
    for(int k=0;k<4;k++){ const unsigned idx=lane+64*k, dd=divm<683,13>(idx), fr=(idx-dd*12)*2, reg=(fr&3)+4*(fr>>3); const unsigned o=mad24(dd,36,((fr>>2)&1)*18+reg*2);
      wa[2*k]=ldsb+K1_WPHI+3712+w*1024+((reg<9 && dd<18)? o:648); wa[2*k+1]=ldsb+K1_WPHI+3712+w*1024+((reg+1<9 && dd<18)? o+2:648); }
    asm volatile("" : "+v"(acc[0][0]), "+v"(acc[0][1]), "+v"(acc[1][0]), "+v"(acc[1][1]), "+v"(cs4), "+v"(sel[0]), "+v"(sel[1]), "+v"(bx[0]), "+v"(bx[1]), "+v"(bx[2]), "+v"(bx[3]), "+v"(bw[0]), "+v"(bw[1]), "+v"(warm));
    asm volatile("" : "+v"(wa[0]), "+v"(wa[1]), "+v"(wa[2]), "+v"(wa[3]), "+v"(wa[4]), "+v"(wa[5]), "+v"(wa[6]), "+v"(wa[7]));
    A_W(1,0,4)
    unsigned wq[2];
    { const unsigned r15=divm<69,10>(lane), c15=lane-r15*15; const unsigned v=mad24(r15,3456,mad24(divm<52,8>(c15),512,c15*128))|(lane<WQW? 0:0x80000000u);
      bload32<0,false>(wq[0], v, srdw, (i*18+w*4)*3456+(xq&1)*512); bload32<0,false>(wq[1], (w==0 && lane<30)? v:0x80000000u, srdw, (i*18+16)*3456+(xq&1)*512); }
    unsigned tf= (w==1 && lane<4)? WS_FLAGS+blockIdx.x*512+lane*128 : 0x80000000u;
    asm volatile("buffer_load_dword %0, %0, %1, %2 offen sc1" : "+v"(tf) : "s"(srdws), "s"(NOPT) : "memory");
    unsigned cw;
    { u64 pc; asm volatile("s_getpc_b64 %0" : "=s"(pc)); const u32x4 srdc=make_srd((const void*)(pc&~127ull), CODE_BYTES);
      bload32<0,false>(cw, (lane<13 && w==3)? lane*128 : 0x80000000u, srdc, i*13*128); }
    asm volatile("s_waitcnt vmcnt(15)" : "+v"(vtag), "+v"(wr0), "+v"(wr1) :: "memory");
    {
      const unsigned a=ldsb+K1_WPHI+w*912+lane*4; unsigned v[4];
      #pragma unroll
      for(int k=0;k<4;k++) v[k]=*LDSP(u16,wa[2*k])|((unsigned)*LDSP(u16,wa[2*k+1])<<16);
      *LDSP(unsigned,a)=v[0]; *LDSP(unsigned,a+256)=v[1]; *LDSP(unsigned,a+512)=v[2]; if(lane<36) *LDSP(unsigned,a+768)=v[3]; }
    tag=rfl(vtag)*0x9E3779B1u+0x7F4A7C15u; vtag=tag; asm volatile("" : "+v"(vtag));
    { float s=0.f;
      #pragma unroll
      for(int k=0;k<4;k++){ s=dot2(wr0[k],0x3f803f80u,s); s=dot2(wr1[k],0x3f803f80u,s); }
      s=dpp_add<0xB1>(s); s=dpp_add<0x4E>(s); s=dpp_add<0x141>(s); s=dpp_add<0x140>(s); s=rows_sum(s);
      const unsigned hi=pk2(s,0.f)&0xffffu; const unsigned lo=pk2(s-lo2f(hi),0.f);
      const unsigned hd=(worw*3641u)>>16, dd=worw-hd*18;
      const unsigned a= dd<16? hd : 32+(hd>>3), hp= dd<16? dd>>3 : (hd>>2)&1, j= dd<16? dd&7 : 2*(hd&3)+(dd-16);
      bstore64_sc1(u32x2{hi|(lo<<16),vtag}, (lane || worw>=576)? 0x80000000u:0u, srdws, WS_WOROW+(32*(a>>1)+16*(j>>2)+4*(2*(a&1)+hp)+(j&3))*8); }
    u32x4 wq0,wq1,wk0,wk1;
    { const unsigned wb=ldsb+K1_WPHI+w*912+blk*32+p*8;
      auto wrow=[&](int dd){ return wb+((unsigned)dd<18u? dd:18)*48; };
      const int R=4*h+q;
      wk0=trfrag(wrow(R>=4? R-4-off:99),wrow(R+4-off)); wk1=trfrag(wrow(R+12-off),wrow(99));
      wq0=trfrag(wrow(R-off),wrow(R+8-off)); wq1=trfrag(wrow(R<4? R+16-off:99),wrow(99)); }
    asm volatile("s_waitcnt vmcnt(9) lgkmcnt(0)\n\ts_barrier" : "+v"(wq0), "+v"(wq1), "+v"(wk0), "+v"(wk1) :: "memory");
    u32x4 xf[2][2][2], st[2]; u32x2 al[2][4], ah[2][4];
    const unsigned bst=ldsb+w*2048+vl;
    #define K1_X(B,E,TT,XB,E2) xf[B][E][TT]=*LDSP(u32x4,bx[2*(E2)+(E)]+(XB)*8192+(TT)*4096);
    #define K1_A(B,I,SL) { al[B][I]=trb(bw[(I)&1]+k1_slot(SL)+((I)>>1)*6912); ah[B][I]=trb(bw[(I)&1]+k1_slot(SL)+((I)>>1)*6912+1728); }
    K1_X(0,0,0,0,0) K1_X(0,0,1,0,0) K1_A(0,0,0) K1_A(0,1,0) K1_X(0,1,0,0,0) K1_X(0,1,1,0,0) K1_A(0,2,0) K1_A(0,3,0) st[0]=*LDSP(u32x4,bst);
    #define MM(D,I,B) SB(); mv32<false>(D,cat(al[cu][I],ah[cu][I]),B); SB();
    #define MC(D,I) SB(); mv16(cs4,sel[(I)&1],cat(al[cu][I],ah[cu][I])); SB();
    #define WL(K) if(cu==0 || (K)<3) glds_m0(mw0+(((j+5)%6)>>1)*K1_PAIR+((cu?0:3)+(K))*4096, vw[(cu?0:3)+(K)], srdw, sw);
        #define SD(K) s1[cu]=dot2(st[cu][K],0x3f803f80u,s1[cu]); s2[cu]=dot2(st[cu][K],st[cu][K],s2[cu]);
    #define A_W1(PR,k) glds_m0(mw0+(PR)*K1_PAIR+(k)*4096, vw[k], srdw, sw0+(PR)*64*3456);
    #define A_X1(b,k) glds_m0(ldsb+w*2048+(b)*8192+(k)*1024, (k)? vx1:vx0, srdx, w*16*1152+(b)*128);
    #define LD(g) if(FP && j==0){ if(g<3){ A_W1(1,4+(g)%3) } else if(g<5){ A_X1(2,((g)-3)&1) } else if(g<8){ A_W1(2,((g)-5)%3) } else { WL((g)&3) } } \
      else if(FP && j<4){ if(g<4){ A_X1(2*j+1+((g)>>1),(g)&1) } else if(!((g)&1)){ WL((((g)-4)>>1)&3) } } else if((g)%3==0){ WL((g)/3) }
    auto pass=[&](auto fp, int s6, unsigned swarm){ constexpr bool FP=decltype(fp)::value;
      #pragma unroll
      for(int j=0;j<6;j++){
        const int s=s6+j; const int cu=j&1, n=cu^1;
        const unsigned sw=sw0+((s+5)>>1)*64*3456;

        MM(acc[0][0],0,xf[cu][0][0]) LD(0)
        if(!FP && j==4) asm volatile("buffer_load_dword %0, %1, %2, %3 offen" : "+v"(wdst) : "v"(warm), "s"(c.srdw), "s"(swarm) : "memory");
        MM(acc[0][1],0,xf[cu][0][1]) LD(1)
        K1_X(n,0,0,(s+1)>>1,n) K1_X(n,0,1,(s+1)>>1,n) SD(0)
        MC(cs[0],0) LD(2)
        K1_A(n,0,(j+1)%6) SD(1)
        MM(acc[1][0],1,xf[cu][0][0]) LD(3)
        MM(acc[1][1],1,xf[cu][0][1]) LD(4)
        K1_A(n,1,(j+1)%6) SD(2)
        MC(cs[1],1) LD(5)
        K1_X(n,1,0,(s+1)>>1,n) K1_X(n,1,1,(s+1)>>1,n) SD(3)
        MM(acc[0][0],2,xf[cu][1][0]) LD(6)
        MM(acc[0][1],2,xf[cu][1][1]) LD(7)
        K1_A(n,2,(j+1)%6)
        MC(cs[0],2) LD(8)
        K1_A(n,3,(j+1)%6)
        MM(acc[1][0],3,xf[cu][1][0]) LD(9)
        MC(cs[1],3) LD(10)
        st[n]=*LDSP(u32x4,bst+((s+1)>>1)*8192+n*1024);
        MM(acc[1][1],3,xf[cu][1][1]) LD(11)
        asm volatile("s_waitcnt vmcnt(%0)\n\ts_barrier" :: "i"(FP? (j==0?17:j==1?16:j==2?18:j==3?22:14) : cu? 10:7) : "memory");
      }
    };
    pass(std::true_type{},0,0u);
    asm volatile("" :: "v"(cw), "v"(tf), "v"(wq[0]), "v"(wq[1]));
    #pragma unroll 1
    for(int s6=6;s6<18;s6+=6){
      unsigned swarm; asm volatile("s_cmp_eq_u32 %1, 12\n\ts_cselect_b32 %0, %2, 0x80000000" : "=s"(swarm) : "s"(s6), "s"((unsigned)(fh*6<W1R? xcd*1152+((T*2+isB)*W1R+fh*6)*9216 : 0x80000000u)) : "scc");
      pass(std::false_type{},s6,swarm); }
    #undef LD
    #undef A_W1
    #undef A_X1
    #undef A_X
    #undef MM
    #undef SD
    #undef K1_X
    #undef K1_A
    #undef WL
    #undef A_W
    { const int row=w*16+l15;
      #pragma unroll
      for(int j=0;j<5;j++){ const unsigned cb=cg*144+32*j+8*(j<4? g:g&1); xr[j]=*LDSP(u32x2,ldsb+(cb>>7)*8192+row*128+((((cb>>4)&7)^((row>>1)&7))*16)+(cb&8)); } }
    asm volatile("s_nop 7" : "+v"(cs4));
    if(lane<16){
      #pragma unroll
      for(int k=0;k<4;k++) *LDSP(float,ldsb+K1_STAT+512+w*256+k*64+lane*4)=cs4[k]; }
    #pragma unroll
    for(int j=0;j<2;j++){ float a=s1[j], b=s2[j];
      a=dpp_add<0xB1>(a); b=dpp_add<0xB1>(b); a=dpp_add<0x4E>(a); b=dpp_add<0x4E>(b); a=dpp_add<0x141>(a); b=dpp_add<0x141>(b);
      const float m=a*(1.f/D_), r=__builtin_amdgcn_rsqf(fmaxf(b*(1.f/D_)-m*m,0.f)+1e-5f);
      *LDSP(f32x2,ldsb+K1_STAT+(w*16+j*8+(lane>>3))*8)=f32x2{-m,r}; }
    asm volatile("s_waitcnt lgkmcnt(0)\n\ts_barrier" : "+v"(xr[0]), "+v"(xr[1]), "+v"(xr[2]), "+v"(xr[3]), "+v"(xr[4]) :: "memory");
    asm volatile("" : "+v"(acc[0][0]), "+v"(acc[0][1]), "+v"(acc[1][0]), "+v"(acc[1][1]));
    f32x16 cs[2];
    #pragma unroll
    for(int t=0;t<2;t++)
      #pragma unroll
      for(int k=0;k<4;k++){ const f32x4 v=*LDSP(f32x4,ldsb+K1_STAT+512+w*256+t*128+k*32+h*16); cs[t][4*k]=v[0]; cs[t][4*k+1]=v[1]; cs[t][4*k+2]=v[2]; cs[t][4*k+3]=v[3]; }
    const f32x2 mr[2]={*LDSP(f32x2,ldsb+K1_STAT+(lane&31)*8), *LDSP(f32x2,ldsb+K1_STAT+256+(lane&31)*8)};
    mx=-*LDSP(float,ldsb+K1_STAT+(w*16+l15)*8);
    const unsigned PK=ldsb+w*10240, VV=PK+5120+64, KT=VV+3072;
    u32x4 f00[2], f01[2], f10[2]; f32x16 pk[2], pq[2];
    #pragma unroll
    for(int t=0;t<2;t++){
      acc[0][t]=__builtin_elementwise_fma(cs[0],(f32x16)mr[t][0],acc[0][t]); acc[1][t]=__builtin_elementwise_fma(cs[1],(f32x16)mr[t][0],acc[1][t]);
      f00[t]=packfrag(acc[0][t],0); f01[t]=packfrag(acc[0][t],1); f10[t]=packfrag(acc[1][t],0); }
    mz32(pk[0],wk0,f01[0]); mz32(pk[1],wk0,f01[1]); mv32<true>(pk[0],wk1,f10[0]); mv32<true>(pk[1],wk1,f10[1]);
    mz32(pq[0],wq0,f00[0]); mz32(pq[1],wq0,f00[1]); mv32<false>(pq[0],wq1,f01[0]); mv32<false>(pq[1],wq1,f01[1]);
    asm volatile("s_nop 7" : "+v"(pk[0]), "+v"(pk[1]));
    #pragma unroll
    for(int t=0;t<2;t++){
      unsigned ok[9]; hedgehog(pk[t],mr[t][1],ok);
      const int tok=32*t+(lane&31); const float r=mr[t][1]; const f32x16& v=acc[1][t];
      const unsigned pa=PK+tok*80+h*40;
      *LDSP(u32x2,pa)=u32x2{ok[0],ok[1]}; *LDSP(u32x2,pa+8)=u32x2{ok[2],ok[3]}; *LDSP(u32x2,pa+16)=u32x2{ok[4],ok[5]}; *LDSP(u32x2,pa+24)=u32x2{ok[6],ok[7]}; *LDSP(u32x2,pa+32)=u32x2{ok[8],0u};
      const unsigned va=VV+tok*48+h*8;
      *LDSP(u32x2,va)=u32x2{pk2(v[4]*r,v[5]*r),pk2(v[6]*r,v[7]*r)}; *LDSP(u32x2,va+16)=u32x2{pk2(v[8]*r,v[9]*r),pk2(v[10]*r,v[11]*r)};
      *LDSP(u32x2,va+32)= h? u32x2{0x3f803f80u,0x3f803f80u} : u32x2{pk2(v[12]*r,v[13]*r),pk2(v[14]*r,v[15]*r)}; }
    { f32x16 kv0, kv1;
      const unsigned pa=PK+(8*h+q)*80+blk*32+p*8, va=VV+(8*h+q)*48+blk*32+p*8;
      #pragma unroll
      for(int s=0;s<4;s++){ const u32x4 b=trfrag(va+s*768,va+s*768+192), a0=trfrag(pa+s*1280,pa+s*1280+320), a1=trfrag(pa+64+s*1280,pa+64+s*1280+320);
        if(s==0){ mz32(kv0,a0,b); mz32(kv1,a1,b); } else { mv32<false>(kv0,a0,b); mv32<false>(kv1,a1,b); } }
      MPAD(kv0,kv1);
      const int vc=lane&31, r19 = vc==20 ? 18 : vc-off; const unsigned dst=KT+r19*80+8*h;
      if((unsigned)(vc-off)<18u || vc==20){ *LDSP(u32x2,dst)=u32x2{pk2(kv0[0],kv0[1]),pk2(kv0[2],kv0[3])}; *LDSP(u32x2,dst+16)=u32x2{pk2(kv0[4],kv0[5]),pk2(kv0[6],kv0[7])};
        *LDSP(u32x2,dst+32)=u32x2{pk2(kv0[8],kv0[9]),pk2(kv0[10],kv0[11])}; *LDSP(u32x2,dst+48)=u32x2{pk2(kv0[12],kv0[13]),pk2(kv0[14],kv0[15])}; *LDSP(u32x2,dst+64)=u32x2{pk2(kv1[0],kv1[1]),pk2(kv1[2],kv1[3])}; }
      const u32x4 t0=*LDSP(u32x4,KT+vl), t1=*LDSP(u32x4,KT+1024+(vl&511));
      const unsigned so=WS_KVP+(head*32+tbA)*1536;
      bstore128_sc1(t0, vl, srdws, so); bstore128_sc1(t1, h? 0x80000000u:vl, srdws, so+1024); }
    asm volatile("" : "+v"(pq[0]), "+v"(pq[1]));
    unsigned oq[2][9];
    #pragma unroll
    for(int t=0;t<2;t++) hedgehog(pq[t],mr[t][1],oq[t]);
    #pragma unroll
    for(int t=0;t<2;t++){ const unsigned so=WS_FQ+(head*64+tbA*2+t)*2304;
      bstore128_sc1(u32x4{oq[t][0],oq[t][1],oq[t][2],oq[t][3]}, vl, srdws, so); bstore128_sc1(u32x4{oq[t][4],oq[t][5],oq[t][6],oq[t][7]}, vl, srdws, so+1024); bstore32_sc1(oq[t][8], lane*4, srdws, so+2048); }
    asm volatile("s_waitcnt vmcnt(0)" : "+v"(wdst));
    flag_store(flags+(32+head)*128+64+tbA, vtag);
  }
  {
    const int head=(i&3)*8+xq, tr=i>>2;
    poll_all(flags+(32+head)*128+64, (lane&31)*4, w, tag);
    BAR();
    u32x4 pv[16], q0[2], q1[2]; unsigned q2[2]; u32x2 wr[3];
    { const unsigned vp= (lane&31)<24? h*1536+(lane&31)*16 : 0x80000000u;
      #pragma unroll
      for(int k=0;k<8;k++){ bload128<0,true>(pv[2*k], vp, srdws, WS_KVP+head*49152+w*384+k*6144); bload128<3072,true>(pv[2*k+1], vp, srdws, WS_KVP+head*49152+w*384+k*6144); } }
    bload64<0,true>(wr[0],tid*8,srdws,WS_WOROW); bload64<2048,true>(wr[1],tid*8,srdws,WS_WOROW); bload64<0,true>(wr[2],(tid&63)*8,srdws,WS_WOROW+4096);
    #pragma unroll
    for(int t=0;t<2;t++){ const unsigned so=WS_FQ+(head*64+tr*8+w*2+t)*2304; bload128<0,true>(q0[t],vl,srdws,so); bload128<1024,true>(q1[t],vl,srdws,so); bload32<2048,true>(q2[t],lane*4,srdws,so); }
    { const int rr10=divm<103,10>(lane), cc10=lane-rr10*10;
      #pragma unroll
      for(int k=0;k<4;k++){ sv[k]=slab_voff<true>(w+4*k,rr10,cc10); sv[4+k]=slab_voff<false>(w+4*(20+k),rr10,cc10); asm volatile("" : "+v"(sv[k]), "+v"(sv[4+k])); } }
    { const int r=lane&31; const unsigned one= r<8? ((r&1)? 0x3f800000u:0x3f80u) : 0u;
      const u32x4 sel={(r>>1)==0?one:0u,(r>>1)==1?one:0u,(r>>1)==2?one:0u,(r>>1)==3?one:0u};
      f32x16 z;
      #pragma unroll
      for(int k=0;k<16;k++){ asm volatile("s_waitcnt vmcnt(%1)" : "+v"(pv[k]) : "i"(24-k)); if(k==0) mz32(z,sel,pv[0]); else mv32<false>(z,sel,pv[k]); }
      asm volatile("s_nop 7\n\ts_nop 3" : "+v"(z));
      const bool zero= w==3 && r>=23;
      if(r<24 || (w==3 && r<28)) *LDSP(u32x2,ldsb+FR_KVB+(24*w+r)*16+8*h)= zero? u32x2{0,0} : u32x2{pk2(z[0],z[1]),pk2(z[2],z[3])}; }
    for(int n=0;n<0x100000;n++){
      asm volatile("s_waitcnt vmcnt(0)" : "+v"(wr[0]), "+v"(wr[1]), "+v"(wr[2]));
      if(__builtin_amdgcn_ballot_w64(wr[0][1]!=tag || wr[1][1]!=tag || wr[2][1]!=tag)==0) break;
      __builtin_amdgcn_s_sleep(2);
      bload64<0,true>(wr[0],tid*8,srdws,WS_WOROW); bload64<2048,true>(wr[1],tid*8,srdws,WS_WOROW); bload64<0,true>(wr[2],(tid&63)*8,srdws,WS_WOROW+4096); }
    asm volatile("s_waitcnt vmcnt(0)" : "+v"(wr[0]), "+v"(wr[1]), "+v"(wr[2]));
    { const unsigned a=ldsb+FR_SLAB+tid*160+144; *LDSP(u32x4,a)=u32x4{wr[0][0],0,0,0}; *LDSP(u32x4,a+256*160)=u32x4{wr[1][0],0,0,0}; if(tid<64) *LDSP(u32x4,a+512*160)=u32x4{wr[2][0],0,0,0}; }
    SLAB(0,SLA)
    asm volatile("s_waitcnt lgkmcnt(0)\n\ts_barrier" ::: "memory");
    const int r=lane&31, hf=(r>>2)&1, reg=(r&3)+4*(r>>3); const int r19 = reg<8 ? 8*hf+reg : reg==10 ? 18 : (reg<10 && !hf) ? 8+reg : 19;
    const unsigned ap=ldsb+FR_KVB+r19*80+h*40;
    const u32x2 b0=*LDSP(u32x2,ap), b1=*LDSP(u32x2,ap+8), b2=*LDSP(u32x2,ap+16), b3=*LDSP(u32x2,ap+24); const u32x4 a0=cat(b0,b1), a1=cat(b2,b3); const unsigned a2=*LDSP(unsigned,ap+32);
    unsigned lo[2];
    #pragma unroll
    for(int t=0;t<2;t++){ f32x16 z;
      asm volatile("" : "+v"(q0[t]), "+v"(q1[t]), "+v"(q2[t]));
      mz32(z,a0,q0[t]); mv32<true>(z,a1,q1[t]); mv32<true>(z,u32x4{a2,0,0,0},u32x4{q2[t],0,0,0}); asm volatile("s_nop 7\n\ts_nop 3" : "+v"(z));
      const float rc=__builtin_amdgcn_rcpf(z[10]+1e-6f);
      const unsigned so=WS_OF+(tr*8+w*2+t)*36*1024;
      bstore128_sc1(u32x4{pk2(z[0]*rc,z[1]*rc),pk2(z[2]*rc,z[3]*rc),pk2(z[4]*rc,z[5]*rc),pk2(z[6]*rc,z[7]*rc)}, vl, srdws, so+head*1024);
      lo[t]=pk2(z[8]*rc,z[9]*rc); }
    bstore32_sc1(__builtin_amdgcn_permlane32_swap(lo[0],lo[1],false,false)[0], lane*4, srdws, WS_OL+((tr*4+w)*32+head)*256);
    SLAB(SLA,SLB)
    WAITV(SLB-SLA);
    flag_store(flags+(tr*4+w)*128+64+head, vtag);
    SLAB(SLB,24)
    #undef SLAB
  }
  WST(0,2)
  poll_all(flags+tb*128+64, (lane&31)*4, w, tag);
  if(w==3) WAITV(0); else WAITV(24);
  BAR();
  if(blockIdx.x==0 && w==3) flag_store(flags+64*128+64, vtag);
  {
    u32x4 o[18];
    { const unsigned vo=(g>>1)*1024+((g&1)*32+(w&1)*16+l15)*16, so=WS_OF+(tb*2+(w>>1))*36*1024;
      #define LD_(k) bload128<((k)&1)*2048,true>(o[k], vo, srdws, so+((k)>>1)*4096);
      LD_(0) LD_(1) LD_(2) LD_(3) LD_(4) LD_(5) LD_(6) LD_(7) LD_(8) LD_(9) LD_(10) LD_(11) LD_(12) LD_(13) LD_(14) LD_(15)
      { const unsigned vq=g*1024+w*64+l15*4;
        #pragma unroll
        for(int k=0;k<2;k++){ unsigned e[4];
          bload32<0,true>(e[0],vq,srdws,WS_OL+tb*8192+k*4096); bload32<256,true>(e[1],vq,srdws,WS_OL+tb*8192+k*4096); bload32<512,true>(e[2],vq,srdws,WS_OL+tb*8192+k*4096); bload32<768,true>(e[3],vq,srdws,WS_OL+tb*8192+k*4096);
          o[16+k]=u32x4{e[0],e[1],e[2],e[3]}; } }
      #undef LD_
    }
    f32x4 acc[5];
    const unsigned wb=ldsb+FR_SLAB+(4*g+q)*160+p*8;
    u32x4 a[3][5];
    #define K2_RD(S) _Pragma("unroll") for(int j=0;j<5;j++) a[(S)%3][j]=trfrag(wb+(S)*5120+j*32, wb+(S)*5120+2560+j*32);
    K2_RD(0) K2_RD(1)
    #pragma unroll
    for(int S=0;S<18;S++){
      if(S+2<18) { K2_RD(S+2) }
      if(S==W2S) bload32<0,false>(warm, warm, c.srdw, fh*6<18-W1R? xcd*1152+(32*W1R+(T*2+isB)*(18-W1R)+fh*6)*9216 : 0x80000000u);
      SB();
      asm volatile("s_waitcnt vmcnt(%1)" : "+v"(o[S]) : "i"((S<16? 23-S : S==16? 4:0)+(S>=W2S)));
      #pragma unroll
      for(int j=0;j<5;j++){ if(S==0) asm volatile("v_mfma_f32_16x16x32_bf16 %0, %1, %2, 0" : "=&v"(acc[j]) : "v"(a[0][j]), "v"(o[0])); else mv16(acc[j],a[S%3][j],o[S]); }
      SB(); }
    #undef K2_RD
    asm volatile("s_nop 7" : "+v"(acc[0]), "+v"(acc[1]), "+v"(acc[2]), "+v"(acc[3]), "+v"(acc[4]));
    BAR();
    const float m=mx+row_bcast2(acc[4][0]+acc[4][1])*(1.f/D_);
    float s2=0.f; unsigned hp[5][2];
    #pragma unroll
    for(int j=0;j<5;j++){ const float v0=acc[j][0]+(lo2f(xr[j][0])-m), v1=acc[j][1]+(hi2f(xr[j][0])-m), v2=acc[j][2]+(lo2f(xr[j][1])-m), v3=acc[j][3]+(hi2f(xr[j][1])-m);
      hp[j][0]=pk2(v0,v1); hp[j][1]=pk2(v2,v3);
      s2+= (j<4 || g<2)? v0*v0+v1*v1+v2*v2+v3*v3 : 0.f; }
    #pragma unroll
    for(int j=0;j<5;j+=2){
      const int j2= j<4? j+1:j; const auto a=__builtin_amdgcn_permlane16_swap(hp[j][0],hp[j2][0],false,false), b=__builtin_amdgcn_permlane16_swap(hp[j][1],hp[j2][1],false,false);
      const unsigned gr=cg*9+2*(j+(g&1))+(g>>1);
      bstore128_sc1(u32x4{a[0],b[0],a[1],b[1]}, (j<4 || g==0)? gr*256+l15*16 : 0x80000000u, srdws, WS_HF+(tb*4+w)*18*1024); }
    s2=rows_sum(s2);
    bstore32_sc1(__float_as_uint(g? m:s2), lane<32? lane*4 : 0x80000000u, srdws, WS_SQ+((tb*8+cg)*4+w)*128);
    asm volatile("s_waitcnt vmcnt(0)" : "+v"(warm));
    flag_store(flags+tb*128+96+cg*4+w, vtag);
  }
  WST(2,6)
  #undef WST
  c.srdact=make_srd(ws+WS_ACT, S_*F_*2);
  const unsigned f2=2*(g&1)+(q>>1);
  const unsigned bfull=ldsb+(8*g+q)*128+(((2*fh)^f2)*32)+p*8, bhalf=ldsb+(8*g+q)*128+(((2*isB+fh)^f2)*32)+p*8;
  #pragma unroll
  for(int k=0;k<3;k++){ c.bA[k]=bfull+k*2*P1_SLOT; c.bB[k]=(bfull^32)+k*2*P1_SLOT; }
  c.stoff=lane*16+th*4096+fh*8192;
  const int fc=w&1, ch=isB;
  d.srdw=make_srd((const char*)wd+(size_t)xcd*576*1152-4096, 576*1152+4096);
  d.srda=make_srd(ws+WS_ACT+(size_t)(T*144+xcd*18)*8192-4096, 18*8192+4096);
  d.m0w=ldsb+w*P2_WREG; d.sowb=(unsigned)(8*w*1152); d.soab=(unsigned)(w*2048);
  auto kst=[&](int s){ if(s==12) return 8+isB; if(s==17) return 9-isB; const int z= s>12? s-1:s, pr=z>>1; const int who = pr<4 ? (pr&1) : (pr>=6); const int j = pr<4 ? (pr>>1) : 2+(pr&1);
    return 2*(((who^isB)?5:0)+((j+T)&3))+(z&1); };
  u64 tabA=0, tabB=0;
  #pragma unroll
  for(int s=1;s<6;s++) tabA|=(u64)kst(s)<<(5*(s-1));
  #pragma unroll
  for(int s=6;s<18;s++) tabB|=(u64)kst(s)<<(5*(s-6));
  asm volatile("" : "+s"(tabA), "+s"(tabB));
  d.sow=d.sowb+kst(0)*36864u; d.soa=d.soab+kst(0)*8192u; d.tab=tabA;
  d.myflag=flags+me*128; d.pflag=flags+(me^8)*128; d.tag=tag; d.vtag=vtag;
  poll_all(flags+T*256+96, (lane&31)*4+(lane>>5)*512, w, tag);
  WAITV(24);
  BAR();
  unsigned sq[2][4];
  { c.vl=lane*16; c.srdx=make_srd(ws+WS_HF+(size_t)(T*8+th*4)*18*1024, 4*18*1024);
    c.vwarm=(4096+((T*2+isB)*18+w*6)*1152+lane*128)|((lane<54 && w<3)? 0:0x80000000u);
    #define XL(k) p1_xload<k,0>(c); p1_xload<k,1>(c); p1_xload<k,2>(c); p1_xload<k,3>(c);
    XL(0) XL(1) XL(2) XL(3) XL(4) XL(5)
    #undef LD
    #undef A_W1
    #undef A_X1
    #pragma unroll
    for(int t=0;t<4;t++){ bload32<0,true>(sq[0][t], l15*4+g*1024, srdws, WS_SQ+(T*2+th)*4096+t*128); bload32<512,true>(sq[1][t], l15*4+g*1024, srdws, WS_SQ+(T*2+th)*4096+t*128); }
    WAITV(28); }
  BAR();
  #pragma unroll
  for(int k=0;k<2;k++){ trread<0>(c.flo[k][0],c.bA[0]+k*4096); trread<512>(c.fhi[k][0],c.bA[0]+k*4096); trread<P1_REG>(c.flo[k][1],c.bA[0]+k*4096); trread<P1_REG+512>(c.fhi[k][1],c.bA[0]+k*4096); }
  unsigned utab=unit_of(0)|unit_of(1)<<4|unit_of(2)<<8|unit_of(3)<<12|4<<16; const unsigned sob=xcd*9*128+rowoff, stb=(T*144+xcd*18)*8192;
  asm volatile("" : "+s"(utab));
  d.srdp=make_srd(ws+WS_PART, 8*S_*D_*2);
  c.so_next=sob+((utab>>4)&15)*128;
  p1_stage<0,0>(c,d,0);
  {
    asm volatile("" : "+v"(sq[0][0]), "+v"(sq[0][1]), "+v"(sq[0][2]), "+v"(sq[0][3]), "+v"(sq[1][0]), "+v"(sq[1][1]), "+v"(sq[1][2]), "+v"(sq[1][3]));
    #pragma unroll
    for(int t=0;t<4;t++){ const float v=rows_sum(__uint_as_float(sq[0][t])+__uint_as_float(sq[1][t]))*(1.f/D_)+1e-5f;
      c.ir2[t]=v; c.c1[t]=-1.4426950408889634f*__builtin_amdgcn_rsqf(v); asm volatile("" : "+v"(c.c1[t]), "+v"(c.ir2[t])); }   }
  p1_stage<1,0>(c,d,0); p1_stage<2,0>(c,d,0); p1_stage<3,0>(c,d,0); p1_stage<4,0>(c,d,0); p1_stage<5,0>(c,d,0);
  c.st_prev=stb+(utab&15)*16384; c.so_cur=c.so_next; utab>>=4;
  #pragma unroll 1
  for(int it=1;it<4;it++){
    const unsigned un=utab&15; utab>>=4;
    c.so_next=sob+(utab&15)*128; d.f1=d.myflag+(utab>>4? 2:0);
    p1_unit<1>(c, d, bfull^(it==3? bfull^bhalf : 0));
    c.st_prev=stb+un*16384; c.so_cur=c.so_next;
  }
  asm volatile("" :: "v"(c.vwarm));
  int ln2=threadIdx.x&63; asm volatile("" : "+v"(ln2));
  { const int cc=ln2&7; const unsigned v0=(ln2>>3)*1152+(((cc>>1)^((ln2>>4)&3))*32)+(cc&1)*16+ch*640+4096;
    #pragma unroll
    for(int j=0;j<4;j++) d.vw[j]=v0-j*896;
    const int c4=ln2&3, r=(ln2>>2)&7; d.vw[4]= ln2<32? r*1152+512+ch*64+(((c4>>1)^((r>>2)&1))*32)+(c4&1)*16+4096 : 0x80000000u;
    d.va=ln2*16+4096-512; }
  p1_unit<2>(c, d, 0);
  c.st_prev=(unsigned)((T*144+xcd*18+8+isB)*8192)+fh;
  #pragma unroll
  for(int j=0;j<7;j++) p2_issue<4>(d,j);
  p2_next(d); d.tab=tabB;
  { asm volatile("" : "+v"(ln2)); const int lane=ln2, q=(lane&15)>>2, p=lane&3; const int g=lane>>4; const int f=2*(g&1)+(q>>1);
    c.stoff=lane*16+th*4096+fh*2048; c.vl=ldsb+K1_STAT+w*1024+lane*8;
    #pragma unroll
    for(int sp=0;sp<3;sp++){
      #pragma unroll
      for(int jj=0;jj<4;jj++) d.ba[sp][jj]=ldsb+sp*2*P2_SLOT+(g>>1)*P2_WREG+fc*2048+(4*(g&1)+q)*128+8*p+((jj^f)*32);
      d.ba[sp][4]=ldsb+sp*2*P2_SLOT+(g>>1)*P2_WREG+4096+(4*(g&1)+q)*64+8*p+((fc^(g&1))*32);
      d.bb[sp]=ldsb+sp*2*P2_SLOT+2*th*P2_WREG+4608+lane*16; } }
  ldsread128a<0>(d.bf[0][0],d.bb[0]); ldsread128a<1024>(d.bf[0][1],d.bb[0]); ldsread128a<P2_WREG>(d.bf[0][2],d.bb[0]); ldsread128a<P2_WREG+1024>(d.bf[0][3],d.bb[0]);
  trread<0>(d.flo[0],d.ba[0][0]); trread<2*P2_WREG>(d.fhi[0],d.ba[0][0]); trread<0>(d.flo[1],d.ba[0][1]); trread<2*P2_WREG>(d.fhi[1],d.ba[0][1]); trread<0>(d.flo[2],d.ba[0][2]); trread<2*P2_WREG>(d.fhi[2],d.ba[0][2]);
  p2_stage<0,true>(d,c);
  p1_take(c);
  d.pv=d.vtag;
  #pragma unroll 1
  for(int s=0;s<2;s++){ d.f3=d.pflag+3*s; p2_stage<1,false>(d,c); p2_stage<2,false>(d,c); p2_stage<3,false>(d,c); p2_stage<4,false>(d,c); p2_stage<5,false>(d,c); p2_stage<0,false>(d,c); }
  asm volatile("" : "+v"(ln2));
  { const int lane=ln2, l15=lane&15, g=lane>>4;
  d.vl=lane*16; d.ownl=ldsb+lane*16;
  #pragma unroll
  for(int u=0;u<2;u++){     unsigned o; asm volatile("s_cmp_eq_u32 %2, %3\n\ts_cselect_b32 %0, 0x80000000, %4\n\ts_cselect_b32 %1, 0, 9216" : "=&s"(d.pb[u]), "=&s"(o) : "s"(2*w+u), "s"(xcd), "s"((unsigned)(((((T*2+ch)*8+2*w+u)*8+xcd)*9)*1024)) : "scc");
    d.ownw[u]=d.ownl+o;
    #pragma unroll
    for(int k=0;k<2;k++){ const int dst=4*th+2*k+u;
      asm volatile("s_cmp_eq_u32 %2, %3\n\ts_cselect_b32 %0, 0x80000000, %4\n\ts_cselect_b32 %1, %5, %6" : "=&s"(d.pb8[u][k]), "=&s"(d.ow8[u][k]) : "s"(dst), "s"(xcd), "s"((unsigned)(((((T*2+ch)*8+dst)*8+xcd)*9)*1024)), "s"(ldsb), "s"(ldsb+9216) : "scc"); } }
  d.vl8=8192+fc*512+lane*8;
  p2_tgroup<0>(d);
  asm volatile("" :: "v"(d.pv));
  p2_tgroup<1>(d);
  const int ow=xcd>>1, ou=xcd&1; const int t16=T*8+(ow>>1)*4+2*ou;
  u32x2 hr[4]; unsigned h8, mA, m8;
  const int ks=ch*10+(ow&1)*4+w;
  { const unsigned vh=(g>>1)*256+l15*16+(g&1)*8, so=WS_HF+(t16*18+ks)*1024, sm=WS_SQ+(t16>>2)*4096+(t16&3)*128+64;
    bload64<0,true>(hr[0], vh, srdws, so); bload64<0,true>(hr[1], vh, srdws, so+18*1024); bload64<512,true>(hr[2], vh, srdws, so); bload64<512,true>(hr[3], vh, srdws, so+18*1024);
    bload32<0,true>(h8, ((lane>>1)&15)*16+(lane>>5)*8+(lane&1)*4, srdws, WS_HF+((t16+(ow&1))*18+8+ch)*1024+w*256);
    bload32<0,true>(mA, l15*4+(lane>>5)*128, srdws, sm); bload32<0,true>(m8, ((lane>>1)&15)*4, srdws, sm+(ow&1)*128); }
  p2_tgroup<2>(d);
  asm volatile("s_nop 7" ::: "memory");
  p2_dump_all<0>(d);
  asm volatile("s_waitcnt vmcnt(0) lgkmcnt(0)\n\ts_barrier" ::: "memory");
  asm volatile("s_mov_b64 exec, 3\n\tglobal_store_dword %0, %1, %2 sc1\n\ts_mov_b64 exec, -1" :: "v"(lane*512), "v"(d.vtag), "s"(flags+(((me&~7)+2*w)*128+32+xcd)) : "memory");
  asm volatile("" : "+v"(hr[0]), "+v"(hr[1]), "+v"(hr[2]), "+v"(hr[3]), "+v"(h8), "+v"(mA), "+v"(m8));
  f32x16 z[2]; float s8[2]; u32x4 idn;
  { const int r=lane&31; const unsigned one= (r>>3)==(lane>>5)? ((r&1)? 0x3f800000u:0x3f80u) : 0u; const int e=(r&7)>>1; idn=u32x4{e==0?one:0u,e==1?one:0u,e==2?one:0u,e==3?one:0u};
    z[0]=(f32x16)__uint_as_float(mA); z[1]=z[0];
    mv32<true>(z[0],idn,cat(hr[0],hr[1])); mv32<true>(z[1],idn,cat(hr[2],hr[3]));
    s8[0]=lo2f(h8)+__uint_as_float(m8); s8[1]=hi2f(h8)+__uint_as_float(m8); }
  u32x4 v[8][2]; unsigned v8[8];
  #pragma unroll
  for(int s=1;s<8;s++) asm volatile("" : "=v"(v[s][0]), "=v"(v[s][1]), "=v"(v8[s]));
  { const unsigned sb=(unsigned)(((T*2+ch)*8+xcd)*8*9*1024);
    unsigned todo=0xfe;
    for(int n=0; n<0x100000 && todo; n++){
      unsigned f; asm volatile("global_load_dword %0, %1, %2 sc1\n\ts_waitcnt vmcnt(0)" : "=v"(f) : "v"(((lane+xcd)&7)*4), "s"(flags+me*128+32) : "memory");
      const unsigned go= n<0xfffff? (unsigned)__builtin_amdgcn_ballot_w64(f==tag)&todo : todo;
      #pragma unroll
      for(int s=1;s<8;s++) if(go>>s&1){ const unsigned so=sb+((xcd+s)&7)*9*1024;
        asm volatile("buffer_load_dwordx4 %0, %3, %5, %6 offen sc1\n\tbuffer_load_dwordx4 %1, %3, %5, %6 offen offset:1024 sc1\n\tbuffer_load_dword %2, %4, %5, %7 offen sc1"
                     : "+v"(v[s][0]), "+v"(v[s][1]), "+v"(v8[s]) : "v"(d.vl), "v"(lane*4), "s"(d.srdp), "s"(so+w*2048), "s"(so+8192+w*256) : "memory"); }
      todo&=~go; }
    v[0][0]=*LDSP(u32x4,d.ownl+w*2048); v[0][1]=*LDSP(u32x4,d.ownl+w*2048+1024); v8[0]=*LDSP(unsigned,ldsb+8192+w*256+lane*4); }
  #pragma unroll
  for(int x_=0;x_<8;x_++){
    if(x_) asm volatile("s_waitcnt vmcnt(0)" : "+v"(v[x_][0]), "+v"(v[x_][1]), "+v"(v8[x_]));
    mv32<false>(z[0],idn,v[x_][0]); mv32<false>(z[1],idn,v[x_][1]);
    s8[0]=dot2(v8[x_],selL,s8[0]); s8[1]=dot2(v8[x_],selH,s8[1]); }
  MPAD(z[0],z[1]);
  { u16* o=out+(size_t)(t16*16+l15+16*(lane>>5))*D_+ks*32+4*(g&1);
    #pragma unroll
    for(int k=0;k<2;k++){ OUTST(u32x2,o+16*k,(u32x2{pk2(z[k][0],z[k][1]),pk2(z[k][2],z[k][3])})); OUTST(u32x2,o+16*k+8,(u32x2{pk2(z[k][4],z[k][5]),pk2(z[k][6],z[k][7])})); }
    OUTST(unsigned,out+(size_t)((t16+(ow&1))*16+((lane>>1)&15))*D_+256+32*ch+8*w+4*(lane>>5)+2*(lane&1),pk2(s8[0],s8[1])); }
  }
}
extern "C" void kernel_launch(void* const* d_in, const int* in_sizes, int n_in, void* d_out, int out_size, void* d_ws, size_t ws_size, hipStream_t stream){
  const u16* x=(const u16*)d_in[0]; const u16* wqkv=(const u16*)d_in[1]; const u16* wphi=(const u16*)d_in[2]; const u16* wo=(const u16*)d_in[3];
  const u16* wg=(const u16*)d_in[5]; const u16* wu=(const u16*)d_in[6]; const u16* wd=(const u16*)d_in[7];
  char* ws=(char*)d_ws+((ws_size-WS_END)&~(size_t)0xfffff);
  (void)hipFuncSetAttribute((const void*)k3, hipFuncAttributeMaxDynamicSharedMemorySize, K_LDS);
  k3<<<256,256,K_LDS,stream>>>(x,wqkv,wphi,wo,wg,wu,wd,ws,(u16*)d_out);
}
```
